```python
import torch

def _softmax(x, axis=-1):
    return torch.softmax(x, dim=axis)

def _gelu(x):
    return torch.nn.functional.gelu(x, approximate='tanh')

def _silu(x):
    return torch.nn.functional.silu(x)

def _sigmoid(x):
    return torch.sigmoid(x)

def _layernorm(x, axis=-1, eps=1e-05):
    m = x.mean(dim=axis, keepdim=True)
    v = x.var(dim=axis, keepdim=True, unbiased=False)
    return (x - m) / torch.sqrt(v + eps)

def _scatter_add(tgt, idx, src, axis=0):
    return tgt.clone().index_add_(axis, idx, src)

def _segment_sum(src, seg, n):
    out = torch.zeros((n,) + src.shape[1:], dtype=src.dtype)
    return out.index_add_(0, seg, src)

def _pad(x, pw):
    flat = tuple((v for pair in reversed(pw) for v in pair))
    return torch.nn.functional.pad(x, flat)
kernel_name = 'adaLN_cond__ssm_conv_h3__glu_gated_bilinear__prompt'

def setup_inputs():

    _w = lambda a: (lambda t: t.to(torch.bfloat16) if t.is_floating_point() else t)(torch.as_tensor(a))

    def _A_setup():
        B, S, D = (1, 2048, 512)
        ks = 4
        return {'q': _w((lambda *s: torch.randn(*s, dtype=torch.bfloat16) * 0.05)(B, S, D)), 'k': _w((lambda *s: torch.randn(*s, dtype=torch.bfloat16) * 0.8)(B, S, D)), 'v': _w((lambda *s: torch.randn(*s, dtype=torch.bfloat16) * 0.8)(B, S, D)), 'shift_k': _w((lambda *s: torch.randn(*s, dtype=torch.bfloat16) * 0.8)(D, ks) * 0.5), 'ssm_k': _w((lambda *s: torch.randn(*s, dtype=torch.bfloat16) * 0.8)(D, S) * (1.0 / S ** 0.5)), 'ssm_D': _w((lambda *s: torch.randn(*s, dtype=torch.bfloat16) * 0.8)(D))}
    _A_in = _A_setup()

    def _B_setup():
        B, S, D, F = (1, 2048, 512, 1792)
        return {'x': _w((lambda *s: torch.randn(*s, dtype=torch.bfloat16) * 0.05)(B, S, D)), 'w_gate': _w((lambda *s: torch.randn(*s, dtype=torch.bfloat16) * 0.8)(D, F) * D ** (-0.5)), 'w_up': _w((lambda *s: torch.randn(*s, dtype=torch.bfloat16) * 0.8)(D, F) * D ** (-0.5)), 'w_down': _w((lambda *s: torch.randn(*s, dtype=torch.bfloat16) * 0.8)(F, D) * F ** (-0.5))}
    _B_in = _B_setup()
    return {'A_q': _A_in['q'], 'A_k': _A_in['k'], 'A_v': _A_in['v'], 'A_shift_k': _A_in['shift_k'], 'A_ssm_k': _A_in['ssm_k'], 'A_ssm_D': _A_in['ssm_D'], 'B_x': _B_in['x'], 'B_w_gate': _B_in['w_gate'], 'B_w_up': _B_in['w_up'], 'B_w_down': _B_in['w_down'], 'c': _w((lambda *s: torch.randn(*s, dtype=torch.bfloat16) * 0.2)(1, 512) * 8.0), 'w_mod': _w((lambda *s: torch.randn(*s, dtype=torch.bfloat16) * 0.2)(512, 6 * 512) * 512 ** (-0.5) * 8.0)}

def reference(A_q, A_k, A_v, A_shift_k, A_ssm_k, A_ssm_D, B_x, B_w_gate, B_w_up, B_w_down, c, w_mod):

    def _A_ref(q, k, v, shift_k, ssm_k, ssm_D):
        B, S, D = q.shape
        ks = shift_k.shape[-1]
        k_pad = _pad(k, ((0, 0), (ks - 1, 0), (0, 0)))
        k_shift = torch.zeros_like(k)
        for t in range(ks):
            k_shift = k_shift + k_pad[:, t:t + S, :] * shift_k[:, ks - 1 - t][None, None, :]
        kv = k_shift * v
        L = 2 * S
        kv_pad = _pad(kv, ((0, 0), (0, S), (0, 0)))
        kf_pad = _pad(ssm_k, ((0, 0), (0, S)))
        kv_f = (lambda x, axes=(-2, -1): torch.fft.rfft2(x.float(), dim=axes))(kv_pad[:, None, :, :], axes=(1, 2))
        kf_f = (lambda x, axes=(-2, -1): torch.fft.rfft2(x.float(), dim=axes))(kf_pad[:, None, :], axes=(1, 2))
        kf_f = (lambda x, *d: x.permute(*d))(kf_f, 1, 2, 0)
        y_f = kv_f * kf_f[None, :, :, :]
        y = (lambda x, s, axes=(-2, -1): torch.fft.irfft2(x, s=s, dim=axes).to(torch.bfloat16))(y_f, (1, L), axes=(1, 2))[:, 0, :S, :]
        y = y + kv * ssm_D[None, None, :]
        return q * y

    def _B_ref(x, w_gate, w_up, w_down):
        g = torch.einsum('bsd,df->bsf', x, w_gate)
        u = torch.einsum('bsd,df->bsf', x, w_up)
        h = g * u
        return torch.einsum('bsf,fd->bsd', h, w_down)
    x = A_q
    mod = (_silu(c) @ w_mod).reshape(c.shape[0], 6, 1, -1)
    sa, ca, ga, sb, cb, gb = (mod[:, 0].clone(), mod[:, 1].clone(), mod[:, 2].clone(), mod[:, 3].clone(), mod[:, 4].clone(), mod[:, 5].clone())
    h = _layernorm(x, axis=-1) * (1 + ca) + sa
    x = x + ga * _A_ref(h, A_k, A_v, A_shift_k, A_ssm_k, A_ssm_D)
    h = _layernorm(x, axis=-1) * (1 + cb) + sb
    x = x + gb * _B_ref(h, B_w_gate, B_w_up, B_w_down)
    return x
```

```cpp
#include <hip/hip_runtime.h>
#include <hip/hip_bf16.h>
#include <stdint.h>
typedef unsigned short u16;
constexpr int S=2048, D=512, F=1792;
__device__ __forceinline__ float b2f(u16 x){ return __uint_as_float(((unsigned)x)<<16); }
__device__ __forceinline__ u16 f2b(float f){ unsigned u=__float_as_uint(f); u += 0x7FFF + ((u>>16)&1); return (u16)(u>>16); }
__device__ __forceinline__ float rb(float f){ return f; }

__global__ void k_mod(const u16* c, const u16* w, float* mod){
  int n = blockIdx.x*blockDim.x+threadIdx.x; if(n>=3072) return;
  float acc=0; for(int k=0;k<512;k++){ float cv=b2f(c[k]); float s=rb(cv/(1.f+__expf(-cv))); acc+=s*b2f(w[k*3072+n]); }
  mod[n]=rb(acc);
}
__global__ void k_kv(const u16* k, const u16* v, const u16* sk, u16* kvT){
  int i = blockIdx.x*blockDim.x+threadIdx.x; int s=i/D, d=i%D;
  float acc=0;
  for(int t=0;t<4;t++){ int ss=s+t-3; float kk = ss>=0? b2f(k[ss*D+d]):0.f; acc = rb(acc + rb(kk*b2f(sk[d*4+3-t]))); }
  kvT[d*S+s]=f2b(acc*b2f(v[i]));
}
__global__ void k_conv(const u16* kvT, const u16* K, const u16* Dv, u16* y2){
  __shared__ float su[S], sk[S];
  int d=blockIdx.x;
  for(int i=threadIdx.x;i<S;i+=blockDim.x){ su[i]=b2f(kvT[d*S+i]); sk[i]=b2f(K[d*S+i]); }
  __syncthreads();
  float dv=b2f(Dv[d]);
  for(int s=threadIdx.x;s<S;s+=blockDim.x){ float a=0; for(int j=0;j<=s;j++) a+=sk[s-j]*su[j]; y2[s*D+d]=f2b(rb(a)+rb(su[s]*dv)); }
}
__device__ float blockSum(float v, float* sm){ for(int o=32;o>0;o>>=1) v+=__shfl_xor(v,o); __syncthreads(); if((threadIdx.x&63)==0) sm[threadIdx.x>>6]=v; __syncthreads(); float r=0; for(int i=0;i<blockDim.x/64;i++) r+=sm[i]; return r; }
__global__ void k_ln(const u16* x, const u16* y2, const float* mod, u16* x1, u16* h2){
  __shared__ float sm[8];
  int s=blockIdx.x, d=threadIdx.x;
  float xv=b2f(x[s*D+d]);
  float m=rb(blockSum(xv,sm)/D);
  float mf=blockSum(xv,sm)/D;
  float var=rb(blockSum((xv-mf)*(xv-mf),sm)/D);
  float ln=rb(rb(xv-m)/rb(sqrtf(rb(var+1e-5f))));
  float h=rb(rb(ln*rb(1.f+mod[512+d]))+mod[d]);
  float a=rb(h*b2f(y2[s*D+d]));
  float xx=rb(xv+rb(mod[1024+d]*a));
  x1[s*D+d]=f2b(xx);
  mf=blockSum(xx,sm)/D; m=rb(mf);
  var=rb(blockSum((xx-mf)*(xx-mf),sm)/D);
  ln=rb(rb(xx-m)/rb(sqrtf(rb(var+1e-5f))));
  h2[s*D+d]=f2b(rb(ln*rb(1.f+mod[2048+d]))+mod[1536+d]);
}
__global__ void k_g1(const u16* h2, const u16* wg, const u16* wu, u16* hh){
  __shared__ float sh[D];
  int s=blockIdx.x; for(int i=threadIdx.x;i<D;i+=blockDim.x) sh[i]=b2f(h2[s*D+i]); __syncthreads();
  for(int f=threadIdx.x;f<F;f+=blockDim.x){ float g=0,u=0; for(int k=0;k<D;k++){ g+=sh[k]*b2f(wg[k*F+f]); u+=sh[k]*b2f(wu[k*F+f]); } hh[s*F+f]=f2b(rb(g)*rb(u)); }
}
__global__ void k_g2(const u16* hh, const u16* wd, const u16* x1, const float* mod, u16* out){
  __shared__ float sh[F];
  int s=blockIdx.x; for(int i=threadIdx.x;i<F;i+=blockDim.x) sh[i]=b2f(hh[s*F+i]); __syncthreads();
  int d=threadIdx.x; float a=0; for(int k=0;k<F;k++) a+=sh[k]*b2f(wd[k*D+d]);
  out[s*D+d]=f2b(b2f(x1[s*D+d])+rb(mod[2560+d]*rb(a)));
}
extern "C" void kernel_launch(void* const* d_in, const int* in_sizes, int n_in, void* d_out, int out_size, void* d_ws, size_t ws_size, hipStream_t stream){
  const u16 *q=(const u16*)d_in[0],*k=(const u16*)d_in[1],*v=(const u16*)d_in[2],*sk=(const u16*)d_in[3],*K=(const u16*)d_in[4],*Dv=(const u16*)d_in[5];
  const u16 *wg=(const u16*)d_in[7],*wu=(const u16*)d_in[8],*wd=(const u16*)d_in[9],*c=(const u16*)d_in[10],*wm=(const u16*)d_in[11];
  char* ws=(char*)d_ws; float* mod=(float*)ws; u16* kvT=(u16*)(ws+(1<<20)); u16* y2=kvT+S*D; u16* x1=y2+S*D; u16* h2=x1+S*D; u16* hh=h2+S*D;
  k_mod<<<12,256,0,stream>>>(c,wm,mod);
  k_kv<<<S*D/256,256,0,stream>>>(k,v,sk,kvT);
  k_conv<<<D,256,0,stream>>>(kvT,K,Dv,y2);
  k_ln<<<S,512,0,stream>>>(q,y2,mod,x1,h2);
  k_g1<<<S,256,0,stream>>>(h2,wg,wu,hh);
  k_g2<<<S,512,0,stream>>>(hh,wd,x1,mod,(u16*)d_out);
}
```

```cpp
#include <hip/hip_runtime.h>
#include <stdint.h>

constexpr unsigned L_P0K = 140800u;
constexpr unsigned L_P0V = 150016u;
constexpr unsigned L_B3U = 49152u;
constexpr unsigned L_KC = 98304u;
constexpr unsigned L_KCS = 4160u;
constexpr unsigned L_KCCH = 16640u;
constexpr unsigned L_U = 131584u;
constexpr unsigned L_UCH = 4608u;
constexpr unsigned L_EX = 98304u;
constexpr unsigned L_STG = 98304u;
constexpr unsigned L_LSTG = 131072u;
constexpr unsigned L_MOD = 147456u;
constexpr unsigned L_MSCR = 153600u;
constexpr unsigned L_ST = 154624u;
constexpr unsigned L_RED = 0u;
constexpr unsigned L_GBP = 49152u;
constexpr unsigned W_EP = 0u;
constexpr unsigned W_CNT = 4096u;
constexpr unsigned W_F1A = 12288u;
constexpr unsigned W_F1B = 45056u;
constexpr unsigned W_F3 = 47104u;
constexpr unsigned W_F4 = 79872u;
constexpr unsigned W_MODP = 473088u;
constexpr unsigned W_DBG = 522240u;
constexpr unsigned W_KVT = 1048576u;
constexpr unsigned W_Y = 3145728u;
constexpr unsigned W_H2 = 7340032u;
constexpr unsigned W_X1 = 5242880u;
constexpr unsigned W_HH = 9568256u;
constexpr unsigned W_B3P = 17039360u;
constexpr unsigned W_HX = 15990784u;
constexpr unsigned W_END = 18612224u;
constexpr unsigned HHS = 50176u;
constexpr unsigned H2S = 17408u;
constexpr unsigned NB4PRE = 2u;
#define STAMP 0
#define GMAP 1
#define ENC -1
#define FINE 0
#define CPF 1
#define WALL 0
#define WARMM 1
#define PRO 0

typedef unsigned u32;
typedef unsigned long long u64;
typedef u32 u32x2 __attribute__((ext_vector_type(2)));
typedef u32 u32x4 __attribute__((ext_vector_type(4)));
typedef u32 u32x8 __attribute__((ext_vector_type(8)));
typedef u32 u32x16 __attribute__((ext_vector_type(16)));
typedef __amdgpu_buffer_rsrc_t rsrc_t;
#define LAS __attribute__((address_space(3)))
#define DI __device__ __forceinline__
constexpr u32 OOB = 0x80000000u;

DI rsrc_t mk(const void* p, u32 bytes) { return __builtin_amdgcn_make_buffer_rsrc((void*)p, 0, bytes, 0x00020000); }
#define TS(k) do { } while (0)

__global__ __launch_bounds__(256, 1) void fused_block(const void* __restrict__ px, const void* __restrict__ pkk, const void* __restrict__ pv,
                                                      const void* __restrict__ psk, const void* __restrict__ pK, const void* __restrict__ pD,
                                                      const void* __restrict__ pwg, const void* __restrict__ pwu, const void* __restrict__ pwd,
                                                      const void* __restrict__ pc, const void* __restrict__ pwm, void* __restrict__ pout,
                                                      void* pws) {
  __shared__ __attribute__((aligned(16))) char lds_[163840];
  asm volatile("" :: "s"((u32)(uintptr_t)(LAS char*)lds_) : "memory");
  TS(0);
  u32 tid = threadIdx.x, b = blockIdx.x, w = __builtin_amdgcn_readfirstlane(tid >> 6);
  u32x2 pad3 = {0, 0};
  u32 pad3b = 0;
  {
    u32 o = (((b >> 3) & 7) * 64 + (tid & 63)) * 64;
    asm volatile(".Lfb_start:\n\ts_getpc_b64 s[84:85]\n\tv_min_u32 %1, .Lfb_end-.Lfb_start-64, %1\n\t"
                 "s_cmp_lg_u32 %2, 3\n\ts_cbranch_scc1 1f\n\tglobal_load_dword %0, %1, s[84:85]\n\t1:"
                 : "=&{v255}"(pad3b), "+v"(o) : "s"(w) : "memory", "scc", "s84", "s85");
  }
  {
  const u32 lane = tid & 63, x = b & 7, i = b >> 3, lin = x * 32 + i;
  asm volatile("" :: "s"(pkk), "s"(pv), "s"(psk), "s"(pK), "s"(pc), "s"(pD), "s"(pwm), "s"(pws), "s"(px), "s"(pwg), "s"(pwu), "s"(pwd), "s"(pout));
  const u32 li = (i >> 2) * 2 + ((i & 3) == 3), task = (0x999999u >> i) & 1u;
  const u32 mt = task ? x * 12 + li : 0, mcg = mt % 48, mkq = mt / 48;
  u32x8 e0; u32x16 c2_; u32x4 e5;
  {
    const rsrc_t rk = mk(pkk, 2u << 20), rv = mk(pv, 2u << 20);
    const u32 base = (64 * i + 8 * w + (lane >> 3)) * 1024 + x * 128;
    const u32 ok = base + (((lane & 7) ^ (2 * ((w + 1) & 3))) * 16), ov = base + (((lane & 7) ^ (2 * w)) * 16);
    const u32 oh = i ? (64 * i - 8 + (lane >> 3)) * 1024 + x * 128 + (lane & 7) * 16 : OOB;
    const u32 mk0 = L_P0K + 1024 + w * 1024, mv0 = L_P0V + w * 1024;
    asm volatile("v_mov_b32 v227, 0\n\tv_mov_b32 v226, %[lin4]\n\t"
                 "global_load_dwordx2 v[224:225], v227, %[pws] sc1\n\tglobal_load_dword v226, v226, %[pD]\n\t"
                 "s_mov_b32 m0, %[mk0]\n\ts_mov_b32 s84, 0x8000\n\t"
                 "buffer_load_dwordx4 %[ok], %[rk], 0 offen lds\n\t"
                 "s_add_u32 m0, %[mk0], 0x1000\n\ts_nop 0\n\t"
                 "buffer_load_dwordx4 %[ok], %[rk], s84 offen lds\n\t"
                 "s_mov_b32 m0, %[mv0]\n\ts_nop 0\n\t"
                 "buffer_load_dwordx4 %[ov], %[rv], 0 offen lds\n\t"
                 "s_add_u32 m0, %[mv0], 0x1000\n\ts_nop 0\n\t"
                 "buffer_load_dwordx4 %[ov], %[rv], s84 offen lds\n\t"
                 "s_cmp_lg_u32 %[w], 0\n\ts_cbranch_scc1 1f\n\t"
                 "s_mov_b32 m0, %[mh]\n\ts_nop 0\n\t"
                 "buffer_load_dwordx4 %[oh], %[rk], 0 offen lds\n\t"
                 "1:"
                 : "=&{v[224:227]}"(e5)
                 : [pws] "s"(pws), [pD] "s"(pD), [lin4] "s"(lin * 4), [ok] "v"(ok), [ov] "v"(ov), [oh] "v"(oh), [rk] "s"(rk), [rv] "s"(rv), [mk0] "s"(mk0), [mv0] "s"(mv0), [w] "s"(w), [mh] "i"(L_P0K)
                 : "memory", "scc", "s84");
    __builtin_amdgcn_sched_barrier(0);
    const u32 ska = (64 * x + 16 * w + 2 * (lane & 7)) * 8, ko = lin * 8192 + tid * 16, kpo = tid ? ko - 8 : OOB, co = (256 * mkq + 64 * w + 8 * (lane >> 3)) * 2;
    const u32 wrmA = lin * (w == 3 ? 8192 : 7168) + lane * 64, wrmB = (w == 3 || lane < 48) ? wrmA + 4096 : OOB;
    const u32 wrmC = w < 3 ? (lin * 3 + w) * 4096 + lane * 64 : OOB;
    asm volatile("s_movk_i32 s84, 0x1000\n\t"
                 "global_load_dwordx4 v[204:207], %[ska], %[psk]\n\t"
                 "buffer_load_dwordx4 v[196:199], %[ko], %[sK], 0 offen\n\t"
                 "buffer_load_dwordx2 v[208:209], %[kpo], %[sK], 0 offen\n\t"
                 "buffer_load_dwordx4 v[200:203], %[ko], %[sK], s84 offen\n\t"
                 "buffer_load_dwordx2 v[210:211], %[kpo], %[sK], s84 offen\n\t"
                 "global_load_dwordx4 v[212:215], %[co], %[pc]\n\t"
                 "buffer_load_dword v254, %[wrmC], %[swf], 0 offen\n\t"
                 : "=&{v[196:203]}"(e0), "=&{v[204:219]}"(c2_), "=&{v[254:255]}"(pad3)
                 : [ska] "v"(ska), [ko] "v"(ko), [kpo] "v"(kpo), [co] "v"(co), [psk] "s"(psk),
                   [pc] "s"(pc), [sK] "s"(mk(pK, 2u << 20)), [wrmA] "v"(wrmA), [wrmB] "v"(wrmB), [wrmC] "v"(wrmC), [swf] "s"(mk(pwm, 3u << 20)),
                   [swr] "s"(mk(w == 0 ? pwg : w == 1 ? pwu : w == 2 ? pwd : px, 2u << 20)), "{v255}"(pad3b)
                 : "memory", "s84");
  }
  const u32 r4 = ((lin >> 1) & 7) + 8 * (lin >> 6) + 32 * ((lin >> 4) & 3);
  u32 rps[2], rpd[2];
#pragma unroll
  for (u32 e = 0; e < 2; ++e) {
    const u32 d = 256 * e + tid, jj = d / 24, rem = d - jj * 24, row = rem / 6, u = (rem - row * 6) >> 1;
    rps[e] = d < 384 ? (4 * r4 + row) * 3584 + 224 * jj + 32 * (u < (jj & 3) ? u : u + 4) + 16 * (rem & 1) : OOB;
    rpd[e] = d < 384 ? W_B3P + (jj * 2 + (lin & 1)) * 49152 + r4 * 384 + rem * 16 : OOB;
  }
  const u32 wmo = (256 * mkq + 64 * w + 8 * (lane >> 3)) * 6144 + mcg * 128 + (lane & 7) * 16;
  TS(21);
  const u32 rg = lane >> 3, cp = lane & 7, ch = 64 * x + 16 * w + 2 * cp, chunk = 2 * w + (cp >> 2), cpos = (cp & 3) * 4;
  const u32 ah = L_P0K + rg * 1024 + ((chunk ^ (2 * (rg & 3))) * 16) + cpos, av = ah + (L_P0V - L_P0K);
  const u32 am = L_P0K + 1024 + rg * 1024 + ((chunk ^ (2 * ((rg + 1) & 3))) * 16) + cpos;
  const u32 kvo = W_KVT + ch * 4096 + (64 * i + 8 * rg) * 2, kcd = L_KC + 32 + tid * 16;
  const u32 zpa = tid < 64 ? L_U + (tid >> 5) * L_UCH + (tid & 31) * 16 : L_KC + ((tid >> 3) & 1) * L_KCCH + ((tid >> 1) & 3) * L_KCS + (tid & 1) * 16;
  const u32 f1w = tid < 8 ? W_F1A + lin * 128 + lane * 16 : OOB, cntz = (i == 0 && w == 0) ? W_CNT + x * 1024 + lane * 16 : OOB;
  const u32 p = 4 * (x >> 1) + (i >> 3), j = 8 * (x & 1) + (i & 7);
  const u32 a2 = 4 * x + (i >> 3), n2 = i & 7;
  const u32 g4 = lane >> 4, q4 = (lane >> 2) & 3, p4 = lane & 3, l16 = lane * 16;
  const u32 f1r = W_F1A + (x * 32 + 8 * w + cp) * 128, mscw = L_MSCR + w * 256 + cp * 32 + (lane >> 5) * 16 + (g4 & 1) * 8;
  const u32 uso = W_KVT + lin * 8192 + w * 1024, um0 = L_U + 512 + w * 1024;
  const u32 ca = L_KC + (w >> 1) * L_KCCH + q4 * L_KCS + 56 - 16 * g4 + 8 * p4 + 256 * (w & 1);
  const u32 cb = L_U + (w >> 1) * L_UCH + 288 + 32 * (lane & 15) + 16 * g4 - 256 * (w & 1);
  const u32 ex = L_EX + w * 8192 + l16, exr = L_EX + w * 2048 + l16, ur = L_U + 512 + w * 1024 + (lane & 15) * 32 + g4 * 8;
  const u32 wl = lin * 32 + (lane >> 1), wj = wl & 15;
  const u32 wrm = w < 2 ? (wl >> 4) * 3584 + 224 * wj + 32 * (wj & 3) + (lane & 1) * 64 : lane < 56 ? (lin * 2 + (w & 1)) * 3584 + lane * 64 : OOB;
  const u32 f1o = W_F1B + (lane < 48 ? lane : 0) * 16, pad1 = 0;
  const u32 msa = L_MSCR + lane * 4;
  const u32 mso = (task && w == 1) ? W_MODP + (mkq * 3072 + mcg * 64 + lane) * 4 : OOB, mfo = (task && tid == 64) ? W_F1B + mt * 8 : OOB;
  const u32 yst = W_Y + (16 * w + ((lane & 15) >> 1)) * 32768 + lin * 128 + (lane & 1) * 64 + g4 * 16;
  const u32 cnto = tid < 8 ? W_CNT + x * 1024 + lane * 128 : OOB;
  const u32 mpo = tid * 16;
  const u32 gp = rg & 1, c0 = 128 * w + 32 * g4 + 8 * gp;
  const u32 xo = (8 * lin + cp) * 1024 + c0 * 2;
  const u32 stw = L_ST + (lane >> 5) * 128 + cp * 16 + w * 4;
  const u32 rps0 = rps[0], rps1 = rps[1], rpd0 = rpd[0], rpd1 = rpd[1], b3so = W_B3P + j * 98304 + w * 3072;
  const u32 dmo = (((cp >> 1) ^ g4) * 32) + (lane & 1) * 16;
  const u32 wdo = rg * 1024 + n2 * 128 + dmo, wdso = w * 448 * 1024, stg = L_STG + w * 12288, wv = w, spare = b;
  const rsrc_t rw = mk(pws, W_END), rg_ = mk(pwg, 512 * 3584), ru_ = mk(pwu, 512 * 3584), rd_ = mk(pwd, 1792 * 1024), rx_ = mk(px, 2u << 20);
  const u32 pat = (4 * g4 + q4) * 128 + ((2 * g4 + (q4 >> 1)) & 3) * 32 + 8 * p4;
  const u64 qw = (u64)pws, qb = (u64)(w < 2 ? pwg : pwu), qd = (u64)pwd;
  const u32x8 ptrs = {(u32)qw, (u32)(qw >> 32), (u32)qb, (u32)(qb >> 32), (u32)qd, (u32)(qd >> 32), 0u, 0u};
  const u32 sx_ = x, si_ = i, slin = lin, sp_ = p, sj_ = j, sa2 = a2, sn2 = n2, spad = 0;
  asm volatile(
    "s_waitcnt lgkmcnt(0)\n\t"
    "s_waitcnt vmcnt(11)\n\t"
    "v_readfirstlane_b32 s56, v224\n\t"
    "v_readfirstlane_b32 s57, v225\n\t"
    "v_readfirstlane_b32 s58, v226\n\t"
    "s_nop 3\n\t"
    "s_add_u32 s56, s56, 0x7f4a7c15\n\t"
    "s_addc_u32 s57, s57, 0x9e3779b9\n\t"
    "s_movk_i32 s84, 0x1000\n\t"
    "v_mov_b32 v36, 0\n\t"
    "v_mov_b32 v37, 0\n\t"
    "v_mov_b32 v38, 0\n\t"
    "v_mov_b32 v39, 0\n\t"
    "v_mov_b64 v[228:229], s[56:57]\n\t"
    "v_mov_b64 v[230:231], s[56:57]\n\t"
    "ds_write_b128 v250, v[36:39]\n\t"
    "buffer_store_dwordx4 v[36:39], v251, %[sw], 0 offen sc1\n\t"
    "s_mov_b32 s85, 34816\n\t"
    "buffer_store_dwordx4 v[36:39], v251, %[sw], s85 offen sc1\n\t"
    "s_nop 1\n\t"
    "s_mov_b64 s[90:91], s[56:57]\n\t"
    "s_add_u32 s90, s90, 32\n\t"
    "s_addc_u32 s91, s91, 0\n\t"
    "s_waitcnt vmcnt(8)\n\t"
    "buffer_load_dwordx4 v[128:131], v8, %[srp], 0 offen\n\t"
    "buffer_load_dwordx4 v[220:223], v9, %[srp], 0 offen\n\t"
    "s_movk_i32 s84, 0x1000\n\t"
    "s_waitcnt lgkmcnt(0)\n\t"
    "s_barrier\n\t"
    "ds_read_b32 v36, v240 offset:640\n\t"
    "ds_read_b32 v37, v240 offset:768\n\t"
    "ds_read_b32 v38, v240 offset:896\n\t"
    "ds_read_b32 v39, v241 offset:0\n\t"
    "ds_read_b32 v40, v241 offset:128\n\t"
    "ds_read_b32 v41, v241 offset:256\n\t"
    "ds_read_b32 v42, v241 offset:384\n\t"
    "ds_read_b32 v43, v241 offset:512\n\t"
    "ds_read_b32 v44, v241 offset:640\n\t"
    "ds_read_b32 v45, v241 offset:768\n\t"
    "ds_read_b32 v46, v241 offset:896\n\t"
    "ds_read_b32 v47, v242 offset:0\n\t"
    "ds_read_b32 v48, v242 offset:128\n\t"
    "ds_read_b32 v49, v242 offset:256\n\t"
    "ds_read_b32 v50, v242 offset:384\n\t"
    "v_lshlrev_b32 v56, 16, v204\n\t"
    "v_lshlrev_b32 v57, 16, v206\n\t"
    "v_and_b32 v58, 0xffff0000, v204\n\t"
    "v_and_b32 v59, 0xffff0000, v206\n\t"
    "v_lshlrev_b32 v60, 16, v205\n\t"
    "v_lshlrev_b32 v61, 16, v207\n\t"
    "v_and_b32 v62, 0xffff0000, v205\n\t"
    "v_and_b32 v63, 0xffff0000, v207\n\t"
    "s_waitcnt lgkmcnt(11)\n\t"
    "v_lshlrev_b32 v64, 16, v36\n\t"
    "v_and_b32 v65, 0xffff0000, v36\n\t"
    "v_lshlrev_b32 v66, 16, v37\n\t"
    "v_and_b32 v67, 0xffff0000, v37\n\t"
    "v_lshlrev_b32 v68, 16, v38\n\t"
    "v_and_b32 v69, 0xffff0000, v38\n\t"
    "v_lshlrev_b32 v70, 16, v39\n\t"
    "v_and_b32 v71, 0xffff0000, v39\n\t"
    "ds_read_b32 v51, v242 offset:512\n\t"
    "ds_read_b32 v52, v242 offset:640\n\t"
    "ds_read_b32 v53, v242 offset:768\n\t"
    "ds_read_b32 v54, v242 offset:896\n\t"
    "v_pk_mul_f32 v[102:103], v[64:65], v[62:63]\n\t"
    "v_pk_mul_f32 v[104:105], v[66:67], v[62:63]\n\t"
    "v_pk_mul_f32 v[106:107], v[68:69], v[62:63]\n\t"
    "v_pk_mul_f32 v[108:109], v[70:71], v[62:63]\n\t"
    "s_waitcnt lgkmcnt(8)\n\t"
    "v_lshlrev_b32 v72, 16, v40\n\t"
    "v_and_b32 v73, 0xffff0000, v40\n\t"
    "v_lshlrev_b32 v74, 16, v41\n\t"
    "v_and_b32 v75, 0xffff0000, v41\n\t"
    "v_lshlrev_b32 v76, 16, v42\n\t"
    "v_and_b32 v77, 0xffff0000, v42\n\t"
    "v_lshlrev_b32 v78, 16, v43\n\t"
    "v_and_b32 v79, 0xffff0000, v43\n\t"
    "v_lshlrev_b32 v80, 16, v44\n\t"
    "v_and_b32 v81, 0xffff0000, v44\n\t"
    "v_lshlrev_b32 v82, 16, v45\n\t"
    "v_and_b32 v83, 0xffff0000, v45\n\t"
    "v_lshlrev_b32 v84, 16, v46\n\t"
    "v_and_b32 v85, 0xffff0000, v46\n\t"
    "v_pk_mul_f32 v[110:111], v[72:73], v[62:63]\n\t"
    "v_pk_mul_f32 v[112:113], v[74:75], v[62:63]\n\t"
    "v_pk_mul_f32 v[114:115], v[76:77], v[62:63]\n\t"
    "v_pk_mul_f32 v[116:117], v[78:79], v[62:63]\n\t"
    "v_pk_fma_f32 v[102:103], v[66:67], v[60:61], v[102:103]\n\t"
    "v_pk_fma_f32 v[104:105], v[68:69], v[60:61], v[104:105]\n\t"
    "v_pk_fma_f32 v[106:107], v[70:71], v[60:61], v[106:107]\n\t"
    "v_pk_fma_f32 v[108:109], v[72:73], v[60:61], v[108:109]\n\t"
    "v_pk_fma_f32 v[110:111], v[74:75], v[60:61], v[110:111]\n\t"
    "v_pk_fma_f32 v[112:113], v[76:77], v[60:61], v[112:113]\n\t"
    "v_pk_fma_f32 v[114:115], v[78:79], v[60:61], v[114:115]\n\t"
    "v_pk_fma_f32 v[116:117], v[80:81], v[60:61], v[116:117]\n\t"
    "v_pk_fma_f32 v[102:103], v[68:69], v[58:59], v[102:103]\n\t"
    "v_pk_fma_f32 v[104:105], v[70:71], v[58:59], v[104:105]\n\t"
    "v_pk_fma_f32 v[106:107], v[72:73], v[58:59], v[106:107]\n\t"
    "v_pk_fma_f32 v[108:109], v[74:75], v[58:59], v[108:109]\n\t"
    "v_pk_fma_f32 v[110:111], v[76:77], v[58:59], v[110:111]\n\t"
    "v_pk_fma_f32 v[112:113], v[78:79], v[58:59], v[112:113]\n\t"
    "v_pk_fma_f32 v[114:115], v[80:81], v[58:59], v[114:115]\n\t"
    "v_pk_fma_f32 v[116:117], v[82:83], v[58:59], v[116:117]\n\t"
    "v_pk_fma_f32 v[102:103], v[70:71], v[56:57], v[102:103]\n\t"
    "v_pk_fma_f32 v[104:105], v[72:73], v[56:57], v[104:105]\n\t"
    "v_pk_fma_f32 v[106:107], v[74:75], v[56:57], v[106:107]\n\t"
    "v_pk_fma_f32 v[108:109], v[76:77], v[56:57], v[108:109]\n\t"
    "v_pk_fma_f32 v[110:111], v[78:79], v[56:57], v[110:111]\n\t"
    "v_pk_fma_f32 v[112:113], v[80:81], v[56:57], v[112:113]\n\t"
    "v_pk_fma_f32 v[114:115], v[82:83], v[56:57], v[114:115]\n\t"
    "v_pk_fma_f32 v[116:117], v[84:85], v[56:57], v[116:117]\n\t"
    "s_waitcnt lgkmcnt(0)\n\t"
    "v_lshlrev_b32 v86, 16, v47\n\t"
    "v_and_b32 v87, 0xffff0000, v47\n\t"
    "v_lshlrev_b32 v88, 16, v48\n\t"
    "v_and_b32 v89, 0xffff0000, v48\n\t"
    "v_lshlrev_b32 v90, 16, v49\n\t"
    "v_and_b32 v91, 0xffff0000, v49\n\t"
    "v_lshlrev_b32 v92, 16, v50\n\t"
    "v_and_b32 v93, 0xffff0000, v50\n\t"
    "v_lshlrev_b32 v94, 16, v51\n\t"
    "v_and_b32 v95, 0xffff0000, v51\n\t"
    "v_lshlrev_b32 v96, 16, v52\n\t"
    "v_and_b32 v97, 0xffff0000, v52\n\t"
    "v_lshlrev_b32 v98, 16, v53\n\t"
    "v_and_b32 v99, 0xffff0000, v53\n\t"
    "v_lshlrev_b32 v100, 16, v54\n\t"
    "v_and_b32 v101, 0xffff0000, v54\n\t"
    "buffer_store_dwordx4 v[228:231], v243, %[sw], 0 offen sc1\n\t"
    "v_pk_mul_f32 v[102:103], v[102:103], v[86:87]\n\t"
    "v_pk_mul_f32 v[104:105], v[104:105], v[88:89]\n\t"
    "v_pk_mul_f32 v[106:107], v[106:107], v[90:91]\n\t"
    "v_pk_mul_f32 v[108:109], v[108:109], v[92:93]\n\t"
    "v_pk_mul_f32 v[110:111], v[110:111], v[94:95]\n\t"
    "v_pk_mul_f32 v[112:113], v[112:113], v[96:97]\n\t"
    "v_pk_mul_f32 v[114:115], v[114:115], v[98:99]\n\t"
    "v_pk_mul_f32 v[116:117], v[116:117], v[100:101]\n\t"
    "v_cvt_pk_bf16_f32 v120, v102, v104\n\t"
    "v_cvt_pk_bf16_f32 v121, v106, v108\n\t"
    "v_cvt_pk_bf16_f32 v122, v110, v112\n\t"
    "v_cvt_pk_bf16_f32 v123, v114, v116\n\t"
    "buffer_store_dwordx4 v[120:123], v248, %[sw], 0 offen sc1\n\t"
    "v_cvt_pk_bf16_f32 v124, v103, v105\n\t"
    "v_cvt_pk_bf16_f32 v125, v107, v109\n\t"
    "v_cvt_pk_bf16_f32 v126, v111, v113\n\t"
    "v_cvt_pk_bf16_f32 v127, v115, v117\n\t"
    "buffer_store_dwordx4 v[124:127], v248, %[sw], s84 offen sc1\n\t"
    "buffer_load_dword v227, v239, %[sw], 0 offen sc1\n\t"
    "s_mov_b32 s85, 0\n\t"
    "buffer_load_dwordx4 v[164:167], v244, %[swm], s85 offen\n\t"
    "s_mov_b32 s85, 6144\n\t"
    "buffer_load_dwordx4 v[168:171], v244, %[swm], s85 offen\n\t"
    "s_mov_b32 s85, 12288\n\t"
    "buffer_load_dwordx4 v[172:175], v244, %[swm], s85 offen\n\t"
    "s_mov_b32 s85, 18432\n\t"
    "buffer_load_dwordx4 v[176:179], v244, %[swm], s85 offen\n\t"
    "s_mov_b32 s85, 24576\n\t"
    "buffer_load_dwordx4 v[180:183], v244, %[swm], s85 offen\n\t"
    "s_mov_b32 s85, 30720\n\t"
    "buffer_load_dwordx4 v[184:187], v244, %[swm], s85 offen\n\t"
    "s_mov_b32 s85, 36864\n\t"
    "buffer_load_dwordx4 v[188:191], v244, %[swm], s85 offen\n\t"
    "s_mov_b32 s85, 43008\n\t"
    "buffer_load_dwordx4 v[192:195], v244, %[swm], s85 offen\n\t"
    "s_movk_i32 s84, 0x1000\n\t"
    "s_waitcnt vmcnt(17)\n\t"
    "v_alignbit_b32 v36, v196, v209, 16\n\t"
    "v_alignbit_b32 v37, v197, v196, 16\n\t"
    "v_alignbit_b32 v38, v198, v197, 16\n\t"
    "v_alignbit_b32 v39, v199, v198, 16\n\t"
    "v_mov_b32 v40, v209\n\t"
    "v_mov_b32 v41, v196\n\t"
    "v_mov_b32 v42, v197\n\t"
    "v_mov_b32 v43, v198\n\t"
    "v_alignbit_b32 v44, v209, v208, 16\n\t"
    "v_alignbit_b32 v45, v196, v209, 16\n\t"
    "v_alignbit_b32 v46, v197, v196, 16\n\t"
    "v_alignbit_b32 v47, v198, v197, 16\n\t"
    "ds_write_b128 v249, v[196:199] offset:0\n\t"
    "ds_write_b128 v249, v[36:39] offset:4160\n\t"
    "ds_write_b128 v249, v[40:43] offset:8320\n\t"
    "ds_write_b128 v249, v[44:47] offset:12480\n\t"
    "v_alignbit_b32 v48, v200, v211, 16\n\t"
    "v_alignbit_b32 v49, v201, v200, 16\n\t"
    "v_alignbit_b32 v50, v202, v201, 16\n\t"
    "v_alignbit_b32 v51, v203, v202, 16\n\t"
    "v_mov_b32 v52, v211\n\t"
    "v_mov_b32 v53, v200\n\t"
    "v_mov_b32 v54, v201\n\t"
    "v_mov_b32 v55, v202\n\t"
    "v_alignbit_b32 v56, v211, v210, 16\n\t"
    "v_alignbit_b32 v57, v200, v211, 16\n\t"
    "v_alignbit_b32 v58, v201, v200, 16\n\t"
    "v_alignbit_b32 v59, v202, v201, 16\n\t"
    "ds_write_b128 v249, v[200:203] offset:16640\n\t"
    "ds_write_b128 v249, v[48:51] offset:20800\n\t"
    "ds_write_b128 v249, v[52:55] offset:24960\n\t"
    "ds_write_b128 v249, v[56:59] offset:29120\n\t"
    "s_cmp_eq_u32 s61, 0\n\t"
    "s_cbranch_scc1 11f\n\t"
    "v_and_b32 v219, 0xffff0000, v215\n\t"
    "v_lshlrev_b32 v218, 16, v215\n\t"
    "v_and_b32 v217, 0xffff0000, v214\n\t"
    "v_lshlrev_b32 v216, 16, v214\n\t"
    "v_and_b32 v215, 0xffff0000, v213\n\t"
    "v_lshlrev_b32 v214, 16, v213\n\t"
    "v_and_b32 v213, 0xffff0000, v212\n\t"
    "v_lshlrev_b32 v212, 16, v212\n\t"
    "v_mul_f32 v60, 0xbfb8aa3b, v212\n\t"
    "v_mul_f32 v61, 0xbfb8aa3b, v213\n\t"
    "v_mul_f32 v62, 0xbfb8aa3b, v214\n\t"
    "v_mul_f32 v63, 0xbfb8aa3b, v215\n\t"
    "v_mul_f32 v64, 0xbfb8aa3b, v216\n\t"
    "v_mul_f32 v65, 0xbfb8aa3b, v217\n\t"
    "v_mul_f32 v66, 0xbfb8aa3b, v218\n\t"
    "v_mul_f32 v67, 0xbfb8aa3b, v219\n\t"
    "v_exp_f32 v60, v60\n\t"
    "v_exp_f32 v61, v61\n\t"
    "v_exp_f32 v62, v62\n\t"
    "v_exp_f32 v63, v63\n\t"
    "v_exp_f32 v64, v64\n\t"
    "v_exp_f32 v65, v65\n\t"
    "v_exp_f32 v66, v66\n\t"
    "v_exp_f32 v67, v67\n\t"
    "v_add_f32 v60, 1.0, v60\n\t"
    "v_add_f32 v61, 1.0, v61\n\t"
    "v_add_f32 v62, 1.0, v62\n\t"
    "v_add_f32 v63, 1.0, v63\n\t"
    "v_add_f32 v64, 1.0, v64\n\t"
    "v_add_f32 v65, 1.0, v65\n\t"
    "v_add_f32 v66, 1.0, v66\n\t"
    "v_add_f32 v67, 1.0, v67\n\t"
    "v_rcp_f32 v60, v60\n\t"
    "v_rcp_f32 v61, v61\n\t"
    "v_rcp_f32 v62, v62\n\t"
    "v_rcp_f32 v63, v63\n\t"
    "v_rcp_f32 v64, v64\n\t"
    "v_rcp_f32 v65, v65\n\t"
    "v_rcp_f32 v66, v66\n\t"
    "v_rcp_f32 v67, v67\n\t"
    "v_mul_f32 v100, v212, v60\n\t"
    "v_mul_f32 v102, v213, v61\n\t"
    "v_mul_f32 v104, v214, v62\n\t"
    "v_mul_f32 v106, v215, v63\n\t"
    "v_mul_f32 v108, v216, v64\n\t"
    "v_mul_f32 v110, v217, v65\n\t"
    "v_mul_f32 v112, v218, v66\n\t"
    "v_mul_f32 v114, v219, v67\n\t"
    "v_cvt_pk_bf16_f32 v100, v100, v100\n\t"
    "v_cvt_pk_bf16_f32 v102, v102, v102\n\t"
    "v_cvt_pk_bf16_f32 v104, v104, v104\n\t"
    "v_cvt_pk_bf16_f32 v106, v106, v106\n\t"
    "v_cvt_pk_bf16_f32 v108, v108, v108\n\t"
    "v_cvt_pk_bf16_f32 v110, v110, v110\n\t"
    "v_cvt_pk_bf16_f32 v112, v112, v112\n\t"
    "v_cvt_pk_bf16_f32 v114, v114, v114\n\t"
    "v_and_b32 v100, 0xffff0000, v100\n\t"
    "v_and_b32 v102, 0xffff0000, v102\n\t"
    "v_and_b32 v104, 0xffff0000, v104\n\t"
    "v_and_b32 v106, 0xffff0000, v106\n\t"
    "v_and_b32 v108, 0xffff0000, v108\n\t"
    "v_and_b32 v110, 0xffff0000, v110\n\t"
    "v_and_b32 v112, 0xffff0000, v112\n\t"
    "v_and_b32 v114, 0xffff0000, v114\n\t"
    "11:\n\t"
    "s_waitcnt vmcnt(9)\n\t"
    "s_waitcnt lgkmcnt(0)\n\t"
    "s_barrier\n\t"
    "buffer_store_dwordx4 v[228:231], v251, %[sw], 0 offen sc1\n\t"
    "s_movk_i32 s84, 0x1000\n\t"
    "ds_read_b64_tr_b16 v[68:69], v232 offset:8\n\t"
    "ds_read_b64_tr_b16 v[70:71], v232 offset:0\n\t"
    "ds_read_b64_tr_b16 v[72:73], v232 offset:520\n\t"
    "ds_read_b64_tr_b16 v[74:75], v232 offset:512\n\t"
    "ds_read_b64_tr_b16 v[76:77], v232 offset:1032\n\t"
    "ds_read_b64_tr_b16 v[78:79], v232 offset:1024\n\t"
    "ds_read_b64_tr_b16 v[80:81], v232 offset:1544\n\t"
    "ds_read_b64_tr_b16 v[82:83], v232 offset:1536\n\t"
    "ds_read_b64_tr_b16 v[84:85], v232 offset:2056\n\t"
    "ds_read_b64_tr_b16 v[86:87], v232 offset:2048\n\t"
    "ds_read_b64_tr_b16 v[88:89], v232 offset:2568\n\t"
    "ds_read_b64_tr_b16 v[90:91], v232 offset:2560\n\t"
    "ds_read_b64_tr_b16 v[92:93], v232 offset:3080\n\t"
    "ds_read_b64_tr_b16 v[94:95], v232 offset:3072\n\t"
    "ds_read_b64_tr_b16 v[96:97], v232 offset:3592\n\t"
    "s_waitcnt lgkmcnt(13)\n\t"
    "ds_read_b64_tr_b16 v[98:99], v232 offset:3584\n\t"
    "ds_read_b64_tr_b16 v[132:133], v232 offset:72\n\t"
    "s_waitcnt lgkmcnt(13)\n\t"
    "ds_read_b64_tr_b16 v[134:135], v232 offset:64\n\t"
    "ds_read_b64_tr_b16 v[136:137], v232 offset:584\n\t"
    "s_waitcnt lgkmcnt(13)\n\t"
    "ds_read_b64_tr_b16 v[138:139], v232 offset:576\n\t"
    "ds_read_b64_tr_b16 v[140:141], v232 offset:1096\n\t"
    "s_waitcnt lgkmcnt(13)\n\t"
    "ds_read_b64_tr_b16 v[142:143], v232 offset:1088\n\t"
    "ds_read_b64_tr_b16 v[144:145], v232 offset:1608\n\t"
    "s_waitcnt lgkmcnt(13)\n\t"
    "ds_read_b64_tr_b16 v[146:147], v232 offset:1600\n\t"
    "ds_read_b64_tr_b16 v[148:149], v232 offset:2120\n\t"
    "s_waitcnt lgkmcnt(13)\n\t"
    "ds_read_b64_tr_b16 v[150:151], v232 offset:2112\n\t"
    "ds_read_b64_tr_b16 v[152:153], v232 offset:2632\n\t"
    "s_waitcnt lgkmcnt(13)\n\t"
    "ds_read_b64_tr_b16 v[154:155], v232 offset:2624\n\t"
    "ds_read_b64_tr_b16 v[156:157], v232 offset:3144\n\t"
    "s_waitcnt lgkmcnt(13)\n\t"
    "ds_read_b64_tr_b16 v[158:159], v232 offset:3136\n\t"
    "ds_read_b64_tr_b16 v[160:161], v232 offset:3656\n\t"
    "s_waitcnt lgkmcnt(13)\n\t"
    "ds_read_b64_tr_b16 v[162:163], v232 offset:3648\n\t"
    "buffer_load_dwordx2 v[52:53], v245, %[sw], 0 offen sc1\n\t"
    "v_lshrrev_b32 v7, 4, v0\n\t"
    "v_lshrrev_b32 v4, 1, v0\n\t"
    "v_and_b32 v18, 0x70, v0\n\t"
    "v_add_u32 v17, 154624, v18\n\t"
    "s_lshl_b32 s84, s60, 9\n\t"
    "s_add_u32 s84, s84, 147456\n\t"
    "v_add_u32 v19, s84, v4\n\t"
    "s_lshl_b32 s84, s60, 11\n\t"
    "s_lshl_b32 s85, s40, 7\n\t"
    "s_add_u32 s84, s84, s85\n\t"
    "s_add_u32 s84, s84, 4096\n\t"
    "v_and_b32 v28, 1, v7\n\t"
    "v_lshl_add_u32 v16, v28, 10, s84\n\t"
    "v_and_b32 v28, 0x30, v7\n\t"
    "v_and_b32 v31, 8, v7\n\t"
    "v_lshl_or_b32 v28, v28, 1, v31\n\t"
    "s_lshl_b32 s84, s60, 8\n\t"
    "s_add_u32 s84, s84, 147456\n\t"
    "v_lshl_add_u32 v24, v28, 1, s84\n\t"
    "v_lshrrev_b32 v25, 2, v18\n\t"
    "v_lshl_add_u32 v25, v28, 6, v25\n\t"
    "s_lshr_b32 s84, s42, 2\n\t"
    "s_lshl_b32 s84, s84, 15\n\t"
    "s_and_b32 s85, s42, 3\n\t"
    "s_lshl_b32 s85, s85, 5\n\t"
    "s_add_u32 s84, s84, s85\n\t"
    "s_lshl_b32 s85, s60, 13\n\t"
    "s_add_u32 s84, s84, s85\n\t"
    "s_add_u32 s84, s84, 3145728\n\t"
    "v_add_u32 v25, s84, v25\n\t"
    "s_lshl_b32 s84, s46, 7\n\t"
    "s_add_u32 s84, s84, 152576\n\t"
    "v_and_b32 v30, 31, v7\n\t"
    "v_lshl_add_u32 v30, v30, 2, s84\n\t"
    "v_lshlrev_b32 v26, 6, v18\n\t"
    "v_lshl_add_u32 v26, v31, 2, v26\n\t"
    "v_and_b32 v28, 32, v7\n\t"
    "v_lshl_add_u32 v26, v28, 1, v26\n\t"
    "v_and_b32 v28, 16, v7\n\t"
    "s_lshl_b32 s84, s42, 13\n\t"
    "s_lshl_b32 s85, s60, 7\n\t"
    "s_add_u32 s84, s84, s85\n\t"
    "s_add_u32 s84, s84, 5242880\n\t"
    "v_add3_u32 v26, v26, v28, s84\n\t"
    "v_and_b32 v28, 0x38, v7\n\t"
    "v_lshl_add_u32 v27, v28, 6, v18\n\t"
    "s_lshr_b32 s84, s42, 1\n\t"
    "s_mul_i32 s84, s84, 17408\n\t"
    "s_and_b32 s85, s42, 1\n\t"
    "s_lshl_b32 s85, s85, 7\n\t"
    "s_add_u32 s84, s84, s85\n\t"
    "s_lshl_b32 s85, s60, 12\n\t"
    "s_add_u32 s84, s84, s85\n\t"
    "s_add_u32 s84, s84, 7340032\n\t"
    "v_add_u32 v27, s84, v27\n\t"
    "s_lshl_b32 s84, s43, 2\n\t"
    "s_add_u32 s84, s84, s60\n\t"
    "s_lshl_b32 s84, s84, 9\n\t"
    "s_add_u32 s84, s84, 47104\n\t"
    "v_and_b32 v28, 3, v7\n\t"
    "v_lshl_add_u32 v29, v28, 7, s84\n\t"
    "s_mul_i32 s84, s44, 224\n\t"
    "s_and_b32 s85, s44, 3\n\t"
    "s_lshl_b32 s85, s85, 5\n\t"
    "s_add_u32 s84, s84, s85\n\t"
    "s_lshl_b32 s85, s46, 7\n\t"
    "s_sub_u32 s84, s84, s85\n\t"
    "v_lshrrev_b32 v28, 3, v7\n\t"
    "v_mul_u32_u24 v28, 2560, v28\n\t"
    "v_add3_u32 v6, v28, v2, s84\n\t"
    "s_and_b32 s84, s60, 1\n\t"
    "s_lshr_b32 s85, s60, 1\n\t"
    "s_mul_i32 s76, s84, 114688\n\t"
    "s_lshl_b32 s84, s84, 12\n\t"
    "s_mul_i32 s83, s85, 16384\n\t"
    "s_add_u32 s83, s83, s84\n\t"
    "s_mul_i32 s77, s85, 49152\n\t"
    "s_add_u32 s77, s77, s84\n\t"
    "s_mul_i32 s82, s60, 3072\n\t"
    "s_waitcnt vmcnt(0)\n\t"
    "v_cmp_ne_u64 vcc, v[52:53], s[56:57]\n\t"
    "s_cmp_eq_u64 vcc, 0\n\t"
    "s_cbranch_scc1 21f\n\t"
    "buffer_load_dwordx2 v[52:53], v245, %[sw], 0 offen sc1\n\t"
    "s_sleep 3\n\t"
    "buffer_load_dwordx2 v[54:55], v245, %[sw], 0 offen sc1\n\t"
    "s_sleep 3\n\t"
    "s_mov_b32 s88, 0x20000\n\t"
    "20:\n\t"
    "buffer_load_dwordx2 v[56:57], v245, %[sw], 0 offen sc1\n\t"
    "s_waitcnt vmcnt(2)\n\t"
    "v_cmp_ne_u64 vcc, v[52:53], s[56:57]\n\t"
    "s_cmp_eq_u64 vcc, 0\n\t"
    "s_cbranch_scc1 21f\n\t"
    "buffer_load_dwordx2 v[52:53], v245, %[sw], 0 offen sc1\n\t"
    "s_waitcnt vmcnt(2)\n\t"
    "v_cmp_ne_u64 vcc, v[54:55], s[56:57]\n\t"
    "s_cmp_eq_u64 vcc, 0\n\t"
    "s_cbranch_scc1 21f\n\t"
    "buffer_load_dwordx2 v[54:55], v245, %[sw], 0 offen sc1\n\t"
    "s_waitcnt vmcnt(2)\n\t"
    "v_cmp_ne_u64 vcc, v[56:57], s[56:57]\n\t"
    "s_cmp_eq_u64 vcc, 0\n\t"
    "s_cbranch_scc1 21f\n\t"
    "s_sub_u32 s88, s88, 1\n\t"
    "s_cmp_lg_u32 s88, 0\n\t"
    "s_cbranch_scc1 20b\n\t"
    "21:\n\t"
    "s_mov_b32 m0, s63\n\t"
    "s_add_u32 s84, s62, 4096\n\t"
    "buffer_load_dwordx4 v0, %[sw], s62 offen sc1 lds\n\t"
    "s_add_u32 m0, s63, 4608\n\t"
    "s_nop 0\n\t"
    "buffer_load_dwordx4 v0, %[sw], s84 offen sc1 lds\n\t"
    "buffer_store_dwordx4 v[128:131], v10, %[sw], 0 offen sc1\n\t"
    "buffer_store_dwordx4 v[220:223], v11, %[sw], 0 offen sc1\n\t"
    "s_movk_i32 s84, 0x1000\n\t"
    "s_cmp_eq_u32 s61, 0\n\t"
    "s_cbranch_scc1 31f\n\t"
    "v_lshlrev_b32 v44, 16, v164\n\t"
    "v_and_b32 v45, 0xffff0000, v164\n\t"
    "v_lshlrev_b32 v46, 16, v165\n\t"
    "v_and_b32 v47, 0xffff0000, v165\n\t"
    "v_lshlrev_b32 v48, 16, v166\n\t"
    "v_and_b32 v49, 0xffff0000, v166\n\t"
    "v_lshlrev_b32 v50, 16, v167\n\t"
    "v_and_b32 v51, 0xffff0000, v167\n\t"
    "v_pk_mul_f32 v[36:37], v[100:101], v[44:45] op_sel_hi:[0,1]\n\t"
    "v_pk_mul_f32 v[38:39], v[100:101], v[46:47] op_sel_hi:[0,1]\n\t"
    "v_pk_mul_f32 v[40:41], v[100:101], v[48:49] op_sel_hi:[0,1]\n\t"
    "v_pk_mul_f32 v[42:43], v[100:101], v[50:51] op_sel_hi:[0,1]\n\t"
    "v_lshlrev_b32 v44, 16, v168\n\t"
    "v_and_b32 v45, 0xffff0000, v168\n\t"
    "v_lshlrev_b32 v46, 16, v169\n\t"
    "v_and_b32 v47, 0xffff0000, v169\n\t"
    "v_lshlrev_b32 v48, 16, v170\n\t"
    "v_and_b32 v49, 0xffff0000, v170\n\t"
    "v_lshlrev_b32 v50, 16, v171\n\t"
    "v_and_b32 v51, 0xffff0000, v171\n\t"
    "v_pk_fma_f32 v[36:37], v[102:103], v[44:45], v[36:37] op_sel_hi:[0,1,1]\n\t"
    "v_pk_fma_f32 v[38:39], v[102:103], v[46:47], v[38:39] op_sel_hi:[0,1,1]\n\t"
    "v_pk_fma_f32 v[40:41], v[102:103], v[48:49], v[40:41] op_sel_hi:[0,1,1]\n\t"
    "v_pk_fma_f32 v[42:43], v[102:103], v[50:51], v[42:43] op_sel_hi:[0,1,1]\n\t"
    "v_lshlrev_b32 v44, 16, v172\n\t"
    "v_and_b32 v45, 0xffff0000, v172\n\t"
    "v_lshlrev_b32 v46, 16, v173\n\t"
    "v_and_b32 v47, 0xffff0000, v173\n\t"
    "v_lshlrev_b32 v48, 16, v174\n\t"
    "v_and_b32 v49, 0xffff0000, v174\n\t"
    "v_lshlrev_b32 v50, 16, v175\n\t"
    "v_and_b32 v51, 0xffff0000, v175\n\t"
    "v_pk_fma_f32 v[36:37], v[104:105], v[44:45], v[36:37] op_sel_hi:[0,1,1]\n\t"
    "v_pk_fma_f32 v[38:39], v[104:105], v[46:47], v[38:39] op_sel_hi:[0,1,1]\n\t"
    "v_pk_fma_f32 v[40:41], v[104:105], v[48:49], v[40:41] op_sel_hi:[0,1,1]\n\t"
    "v_pk_fma_f32 v[42:43], v[104:105], v[50:51], v[42:43] op_sel_hi:[0,1,1]\n\t"
    "v_lshlrev_b32 v44, 16, v176\n\t"
    "v_and_b32 v45, 0xffff0000, v176\n\t"
    "v_lshlrev_b32 v46, 16, v177\n\t"
    "v_and_b32 v47, 0xffff0000, v177\n\t"
    "v_lshlrev_b32 v48, 16, v178\n\t"
    "v_and_b32 v49, 0xffff0000, v178\n\t"
    "v_lshlrev_b32 v50, 16, v179\n\t"
    "v_and_b32 v51, 0xffff0000, v179\n\t"
    "v_pk_fma_f32 v[36:37], v[106:107], v[44:45], v[36:37] op_sel_hi:[0,1,1]\n\t"
    "v_pk_fma_f32 v[38:39], v[106:107], v[46:47], v[38:39] op_sel_hi:[0,1,1]\n\t"
    "v_pk_fma_f32 v[40:41], v[106:107], v[48:49], v[40:41] op_sel_hi:[0,1,1]\n\t"
    "v_pk_fma_f32 v[42:43], v[106:107], v[50:51], v[42:43] op_sel_hi:[0,1,1]\n\t"
    "v_lshlrev_b32 v44, 16, v180\n\t"
    "v_and_b32 v45, 0xffff0000, v180\n\t"
    "v_lshlrev_b32 v46, 16, v181\n\t"
    "v_and_b32 v47, 0xffff0000, v181\n\t"
    "v_lshlrev_b32 v48, 16, v182\n\t"
    "v_and_b32 v49, 0xffff0000, v182\n\t"
    "v_lshlrev_b32 v50, 16, v183\n\t"
    "v_and_b32 v51, 0xffff0000, v183\n\t"
    "v_pk_fma_f32 v[36:37], v[108:109], v[44:45], v[36:37] op_sel_hi:[0,1,1]\n\t"
    "v_pk_fma_f32 v[38:39], v[108:109], v[46:47], v[38:39] op_sel_hi:[0,1,1]\n\t"
    "v_pk_fma_f32 v[40:41], v[108:109], v[48:49], v[40:41] op_sel_hi:[0,1,1]\n\t"
    "v_pk_fma_f32 v[42:43], v[108:109], v[50:51], v[42:43] op_sel_hi:[0,1,1]\n\t"
    "v_lshlrev_b32 v44, 16, v184\n\t"
    "v_and_b32 v45, 0xffff0000, v184\n\t"
    "v_lshlrev_b32 v46, 16, v185\n\t"
    "v_and_b32 v47, 0xffff0000, v185\n\t"
    "v_lshlrev_b32 v48, 16, v186\n\t"
    "v_and_b32 v49, 0xffff0000, v186\n\t"
    "v_lshlrev_b32 v50, 16, v187\n\t"
    "v_and_b32 v51, 0xffff0000, v187\n\t"
    "v_pk_fma_f32 v[36:37], v[110:111], v[44:45], v[36:37] op_sel_hi:[0,1,1]\n\t"
    "v_pk_fma_f32 v[38:39], v[110:111], v[46:47], v[38:39] op_sel_hi:[0,1,1]\n\t"
    "v_pk_fma_f32 v[40:41], v[110:111], v[48:49], v[40:41] op_sel_hi:[0,1,1]\n\t"
    "v_pk_fma_f32 v[42:43], v[110:111], v[50:51], v[42:43] op_sel_hi:[0,1,1]\n\t"
    "v_lshlrev_b32 v44, 16, v188\n\t"
    "v_and_b32 v45, 0xffff0000, v188\n\t"
    "v_lshlrev_b32 v46, 16, v189\n\t"
    "v_and_b32 v47, 0xffff0000, v189\n\t"
    "v_lshlrev_b32 v48, 16, v190\n\t"
    "v_and_b32 v49, 0xffff0000, v190\n\t"
    "v_lshlrev_b32 v50, 16, v191\n\t"
    "v_and_b32 v51, 0xffff0000, v191\n\t"
    "v_pk_fma_f32 v[36:37], v[112:113], v[44:45], v[36:37] op_sel_hi:[0,1,1]\n\t"
    "v_pk_fma_f32 v[38:39], v[112:113], v[46:47], v[38:39] op_sel_hi:[0,1,1]\n\t"
    "v_pk_fma_f32 v[40:41], v[112:113], v[48:49], v[40:41] op_sel_hi:[0,1,1]\n\t"
    "v_pk_fma_f32 v[42:43], v[112:113], v[50:51], v[42:43] op_sel_hi:[0,1,1]\n\t"
    "v_lshlrev_b32 v44, 16, v192\n\t"
    "v_and_b32 v45, 0xffff0000, v192\n\t"
    "v_lshlrev_b32 v46, 16, v193\n\t"
    "v_and_b32 v47, 0xffff0000, v193\n\t"
    "v_lshlrev_b32 v48, 16, v194\n\t"
    "v_and_b32 v49, 0xffff0000, v194\n\t"
    "v_lshlrev_b32 v50, 16, v195\n\t"
    "v_and_b32 v51, 0xffff0000, v195\n\t"
    "v_pk_fma_f32 v[36:37], v[114:115], v[44:45], v[36:37] op_sel_hi:[0,1,1]\n\t"
    "v_pk_fma_f32 v[38:39], v[114:115], v[46:47], v[38:39] op_sel_hi:[0,1,1]\n\t"
    "v_pk_fma_f32 v[40:41], v[114:115], v[48:49], v[40:41] op_sel_hi:[0,1,1]\n\t"
    "v_pk_fma_f32 v[42:43], v[114:115], v[50:51], v[42:43] op_sel_hi:[0,1,1]\n\t"
    "s_nop 0\n\t"
    "v_permlane32_swap_b32 v36, v40\n\t"
    "v_permlane32_swap_b32 v37, v41\n\t"
    "v_permlane32_swap_b32 v38, v42\n\t"
    "v_permlane32_swap_b32 v39, v43\n\t"
    "v_add_f32 v36, v36, v40\n\t"
    "v_add_f32 v37, v37, v41\n\t"
    "v_add_f32 v38, v38, v42\n\t"
    "v_add_f32 v39, v39, v43\n\t"
    "s_nop 0\n\t"
    "v_permlane16_swap_b32 v36, v38\n\t"
    "v_permlane16_swap_b32 v37, v39\n\t"
    "v_add_f32 v36, v36, v38\n\t"
    "v_add_f32 v37, v37, v39\n\t"
    "s_nop 0\n\t"
    "v_add_f32_dpp v36, v36, v36 row_ror:8 row_mask:0xf bank_mask:0xf\n\t"
    "v_add_f32_dpp v37, v37, v37 row_ror:8 row_mask:0xf bank_mask:0xf\n\t"
    "ds_write_b64 v246, v[36:37]\n\t"
    "31:\n\t"
    "s_waitcnt vmcnt(2)\n\t"
    "s_waitcnt lgkmcnt(0)\n\t"
    "s_barrier\n\t"
    "ds_read_b128 v[100:103], v233 offset:192\n\t"
    "ds_read_b128 v[104:107], v233 offset:704\n\t"
    "ds_read_b128 v[108:111], v233 offset:1216\n\t"
    "ds_read_b128 v[112:115], v233 offset:1728\n\t"
    "ds_read_b128 v[116:119], v233 offset:2240\n\t"
    "ds_read_b128 v[120:123], v233 offset:2752\n\t"
    "ds_read_b128 v[124:127], v233 offset:3264\n\t"
    "ds_read_b128 v[128:131], v233 offset:3776\n\t"
    "s_mul_i32 s89, s60, 24576\n\t"
    "s_add_u32 s96, s52, s65\n\t"
    "s_addc_u32 s97, s53, 0\n\t"
    "v_add_u32 v226, 7168, v2\n\t"
    "s_waitcnt lgkmcnt(7)\n\t"
    "v_mfma_f32_16x16x32_bf16 v[36:39], v[68:71], v[100:103], 0\n\t"
    "ds_read_b128 v[164:167], v233 offset:128\n\t"
    "s_add_u32 m0, s89, 0\n\t"
    "s_waitcnt lgkmcnt(7)\n\t"
    "v_mfma_f32_16x16x32_bf16 v[40:43], v[68:71], v[104:107], 0\n\t"
    "ds_read_b128 v[168:171], v233 offset:640\n\t"
    "s_add_u32 s98, s96, 65536\n\t"
    "s_addc_u32 s99, s97, 0\n\t"
    "v_mfma_f32_16x16x32_bf16 v[40:43], v[72:75], v[100:103], v[40:43]\n\t"
    "ds_read_b128 v[172:175], v233 offset:1152\n\t"
    "global_load_lds_dwordx4 v2, s[98:99] offset:0\n\t"
    "s_waitcnt lgkmcnt(8)\n\t"
    "v_mfma_f32_16x16x32_bf16 v[44:47], v[68:71], v[108:111], 0\n\t"
    "ds_read_b128 v[176:179], v233 offset:1664\n\t"
    "ds_read_b32 v240, v237 offset:0\n\t"
    "v_mfma_f32_16x16x32_bf16 v[44:47], v[72:75], v[104:107], v[44:47]\n\t"
    "ds_read_b128 v[180:183], v233 offset:2176\n\t"
    "ds_read_b32 v241, v237 offset:256\n\t"
    "v_mfma_f32_16x16x32_bf16 v[44:47], v[76:79], v[100:103], v[44:47]\n\t"
    "ds_read_b128 v[184:187], v233 offset:2688\n\t"
    "ds_read_b32 v242, v237 offset:512\n\t"
    "s_waitcnt lgkmcnt(13)\n\t"
    "v_mfma_f32_16x16x32_bf16 v[48:51], v[68:71], v[112:115], 0\n\t"
    "ds_read_b128 v[188:191], v233 offset:3200\n\t"
    "ds_read_b32 v243, v237 offset:768\n\t"
    "v_mfma_f32_16x16x32_bf16 v[48:51], v[72:75], v[108:111], v[48:51]\n\t"
    "s_waitcnt lgkmcnt(7)\n\t"
    "ds_read_b128 v[192:195], v233 offset:3712\n\t"
    "v_mfma_f32_16x16x32_bf16 v[48:51], v[76:79], v[104:107], v[48:51]\n\t"
    "global_load_lds_dwordx4 v226, s[98:99] offset:1024\n\t"
    "s_lshl_b32 s84, s43, 2\n\t"
    "v_mfma_f32_16x16x32_bf16 v[48:51], v[80:83], v[100:103], v[48:51]\n\t"
    "s_add_u32 s84, s84, s60\n\t"
    "s_lshl_b32 s84, s84, 4\n\t"
    "v_mfma_f32_16x16x32_bf16 v[52:55], v[68:71], v[116:119], 0\n\t"
    "s_add_u32 s84, s84, s44\n\t"
    "s_lshl_b32 s84, s84, 7\n\t"
    "v_mfma_f32_16x16x32_bf16 v[52:55], v[72:75], v[112:115], v[52:55]\n\t"
    "s_add_u32 s84, s84, 79872\n\t"
    "v_lshrrev_b32 v244, 3, v7\n\t"
    "v_mfma_f32_16x16x32_bf16 v[52:55], v[76:79], v[108:111], v[52:55]\n\t"
    "v_and_b32 v245, 0x70, v0\n\t"
    "v_lshl_add_u32 v244, v244, 17, v245\n\t"
    "v_mfma_f32_16x16x32_bf16 v[52:55], v[80:83], v[104:107], v[52:55]\n\t"
    "v_not_b32 v248, v228\n\t"
    "v_mov_b32 v249, v248\n\t"
    "v_mfma_f32_16x16x32_bf16 v[52:55], v[84:87], v[100:103], v[52:55]\n\t"
    "s_waitcnt lgkmcnt(1)\n\t"
    "v_add_f32 v240, v240, v241\n\t"
    "v_add_f32 v242, v242, v243\n\t"
    "v_add_f32 v240, v240, v242\n\t"
    "buffer_store_dword v240, v238, %[sw], 0 offen sc1\n\t"
    "v_mfma_f32_16x16x32_bf16 v[56:59], v[68:71], v[120:123], 0\n\t"
    "v_mov_b32 v250, v248\n\t"
    "v_mov_b32 v251, v248\n\t"
    "v_mfma_f32_16x16x32_bf16 v[56:59], v[72:75], v[116:119], v[56:59]\n\t"
    "buffer_load_dwordx4 v[196:199], v236, %[sx], 0 offen offset:0\n\t"
    "s_mov_b64 exec, 0xffffff\n\t"
    "buffer_store_dwordx4 v[248:251], v244, %[sw], s84 offen sc1\n\t"
    "s_mov_b64 exec, -1\n\t"
    "v_mfma_f32_16x16x32_bf16 v[56:59], v[76:79], v[112:115], v[56:59]\n\t"
    "v_mfma_f32_16x16x32_bf16 v[56:59], v[80:83], v[108:111], v[56:59]\n\t"
    "v_mfma_f32_16x16x32_bf16 v[56:59], v[84:87], v[104:107], v[56:59]\n\t"
    "v_mfma_f32_16x16x32_bf16 v[56:59], v[88:91], v[100:103], v[56:59]\n\t"
    "v_mfma_f32_16x16x32_bf16 v[60:63], v[68:71], v[124:127], 0\n\t"
    "v_mfma_f32_16x16x32_bf16 v[60:63], v[72:75], v[120:123], v[60:63]\n\t"
    "v_mfma_f32_16x16x32_bf16 v[60:63], v[76:79], v[116:119], v[60:63]\n\t"
    "v_mfma_f32_16x16x32_bf16 v[60:63], v[80:83], v[112:115], v[60:63]\n\t"
    "v_mfma_f32_16x16x32_bf16 v[60:63], v[84:87], v[108:111], v[60:63]\n\t"
    "v_mfma_f32_16x16x32_bf16 v[60:63], v[88:91], v[104:107], v[60:63]\n\t"
    "v_mfma_f32_16x16x32_bf16 v[60:63], v[92:95], v[100:103], v[60:63]\n\t"
    "v_mfma_f32_16x16x32_bf16 v[64:67], v[68:71], v[128:131], 0\n\t"
    "v_mfma_f32_16x16x32_bf16 v[64:67], v[72:75], v[124:127], v[64:67]\n\t"
    "v_mfma_f32_16x16x32_bf16 v[64:67], v[76:79], v[120:123], v[64:67]\n\t"
    "v_mfma_f32_16x16x32_bf16 v[64:67], v[80:83], v[116:119], v[64:67]\n\t"
    "buffer_load_dwordx4 v[200:203], v236, %[sx], 0 offen offset:32\n\t"
    "v_mfma_f32_16x16x32_bf16 v[64:67], v[84:87], v[112:115], v[64:67]\n\t"
    "v_mfma_f32_16x16x32_bf16 v[64:67], v[88:91], v[108:111], v[64:67]\n\t"
    "v_mfma_f32_16x16x32_bf16 v[64:67], v[92:95], v[104:107], v[64:67]\n\t"
    "v_mfma_f32_16x16x32_bf16 v[64:67], v[96:99], v[100:103], v[64:67]\n\t"
    "ds_read_b64_tr_b16 v[68:69], v232 offset:136\n\t"
    "v_mfma_f32_16x16x32_bf16 v[36:39], v[132:135], v[164:167], v[36:39]\n\t"
    "ds_read_b64_tr_b16 v[70:71], v232 offset:128\n\t"
    "v_mfma_f32_16x16x32_bf16 v[40:43], v[132:135], v[168:171], v[40:43]\n\t"
    "ds_read_b128 v[100:103], v233 offset:64\n\t"
    "v_mfma_f32_16x16x32_bf16 v[40:43], v[136:139], v[164:167], v[40:43]\n\t"
    "ds_read_b64_tr_b16 v[72:73], v232 offset:648\n\t"
    "v_mfma_f32_16x16x32_bf16 v[44:47], v[132:135], v[172:175], v[44:47]\n\t"
    "ds_read_b64_tr_b16 v[74:75], v232 offset:640\n\t"
    "s_lshl_b32 s86, s58, 16\n\t"
    "v_mfma_f32_16x16x32_bf16 v[44:47], v[136:139], v[168:171], v[44:47]\n\t"
    "ds_read_b128 v[104:107], v233 offset:576\n\t"
    "s_and_b32 s87, s58, 0xffff0000\n\t"
    "v_mfma_f32_16x16x32_bf16 v[44:47], v[140:143], v[164:167], v[44:47]\n\t"
    "ds_read_b64_tr_b16 v[76:77], v232 offset:1160\n\t"
    "ds_read_b64 v[206:207], v13 offset:0\n\t"
    "v_mfma_f32_16x16x32_bf16 v[48:51], v[132:135], v[176:179], v[48:51]\n\t"
    "ds_read_b64_tr_b16 v[78:79], v232 offset:1152\n\t"
    "ds_read_b64 v[210:211], v13 offset:4608\n\t"
    "v_mfma_f32_16x16x32_bf16 v[48:51], v[136:139], v[172:175], v[48:51]\n\t"
    "ds_read_b128 v[108:111], v233 offset:1088\n\t"
    "ds_read_b64 v[214:215], v13 offset:512\n\t"
    "v_mfma_f32_16x16x32_bf16 v[48:51], v[140:143], v[168:171], v[48:51]\n\t"
    "ds_read_b64_tr_b16 v[80:81], v232 offset:1672\n\t"
    "ds_read_b64 v[218:219], v13 offset:5120\n\t"
    "v_mfma_f32_16x16x32_bf16 v[48:51], v[144:147], v[164:167], v[48:51]\n\t"
    "s_waitcnt lgkmcnt(5)\n\t"
    "ds_read_b64_tr_b16 v[82:83], v232 offset:1664\n\t"
    "v_mfma_f32_16x16x32_bf16 v[52:55], v[132:135], v[180:183], v[52:55]\n\t"
    "ds_read_b128 v[112:115], v233 offset:1600\n\t"
    "buffer_load_dword v227, v235, %[swr], 0 offen offset:0\n\t"
    "v_mfma_f32_16x16x32_bf16 v[52:55], v[136:139], v[176:179], v[52:55]\n\t"
    "ds_read_b64_tr_b16 v[84:85], v232 offset:2184\n\t"
    "v_mfma_f32_16x16x32_bf16 v[52:55], v[140:143], v[172:175], v[52:55]\n\t"
    "ds_read_b64_tr_b16 v[86:87], v232 offset:2176\n\t"
    "v_mfma_f32_16x16x32_bf16 v[52:55], v[144:147], v[168:171], v[52:55]\n\t"
    "ds_read_b128 v[116:119], v233 offset:2112\n\t"
    "v_mfma_f32_16x16x32_bf16 v[52:55], v[148:151], v[164:167], v[52:55]\n\t"
    "ds_read_b64_tr_b16 v[88:89], v232 offset:2696\n\t"
    "v_mfma_f32_16x16x32_bf16 v[56:59], v[132:135], v[184:187], v[56:59]\n\t"
    "ds_read_b64_tr_b16 v[90:91], v232 offset:2688\n\t"
    "v_mfma_f32_16x16x32_bf16 v[56:59], v[136:139], v[180:183], v[56:59]\n\t"
    "ds_read_b128 v[120:123], v233 offset:2624\n\t"
    "v_mfma_f32_16x16x32_bf16 v[56:59], v[140:143], v[176:179], v[56:59]\n\t"
    "ds_read_b64_tr_b16 v[92:93], v232 offset:3208\n\t"
    "v_mfma_f32_16x16x32_bf16 v[56:59], v[144:147], v[172:175], v[56:59]\n\t"
    "s_waitcnt lgkmcnt(7)\n\t"
    "ds_read_b64_tr_b16 v[94:95], v232 offset:3200\n\t"
    "v_mfma_f32_16x16x32_bf16 v[56:59], v[148:151], v[168:171], v[56:59]\n\t"
    "ds_read_b128 v[124:127], v233 offset:3136\n\t"
    "v_lshlrev_b32 v204, 16, v206\n\t"
    "v_mfma_f32_16x16x32_bf16 v[56:59], v[152:155], v[164:167], v[56:59]\n\t"
    "ds_read_b64_tr_b16 v[96:97], v232 offset:3720\n\t"
    "v_and_b32 v205, 0xffff0000, v206\n\t"
    "v_mfma_f32_16x16x32_bf16 v[60:63], v[132:135], v[188:191], v[60:63]\n\t"
    "ds_read_b64_tr_b16 v[98:99], v232 offset:3712\n\t"
    "v_lshlrev_b32 v206, 16, v207\n\t"
    "v_mfma_f32_16x16x32_bf16 v[60:63], v[136:139], v[184:187], v[60:63]\n\t"
    "ds_read_b128 v[128:131], v233 offset:3648\n\t"
    "v_and_b32 v207, 0xffff0000, v207\n\t"
    "v_mfma_f32_16x16x32_bf16 v[60:63], v[140:143], v[180:183], v[60:63]\n\t"
    "v_mul_f32 v204, s86, v204\n\t"
    "v_mul_f32 v205, s86, v205\n\t"
    "v_mfma_f32_16x16x32_bf16 v[60:63], v[144:147], v[176:179], v[60:63]\n\t"
    "v_mul_f32 v206, s86, v206\n\t"
    "v_mul_f32 v207, s86, v207\n\t"
    "v_mfma_f32_16x16x32_bf16 v[60:63], v[148:151], v[172:175], v[60:63]\n\t"
    "v_lshlrev_b32 v208, 16, v210\n\t"
    "v_and_b32 v209, 0xffff0000, v210\n\t"
    "v_mfma_f32_16x16x32_bf16 v[60:63], v[152:155], v[168:171], v[60:63]\n\t"
    "v_lshlrev_b32 v210, 16, v211\n\t"
    "v_and_b32 v211, 0xffff0000, v211\n\t"
    "v_mfma_f32_16x16x32_bf16 v[60:63], v[156:159], v[164:167], v[60:63]\n\t"
    "v_mul_f32 v208, s87, v208\n\t"
    "v_mul_f32 v209, s87, v209\n\t"
    "v_mfma_f32_16x16x32_bf16 v[64:67], v[132:135], v[192:195], v[64:67]\n\t"
    "v_mul_f32 v210, s87, v210\n\t"
    "v_mul_f32 v211, s87, v211\n\t"
    "v_mfma_f32_16x16x32_bf16 v[64:67], v[136:139], v[188:191], v[64:67]\n\t"
    "v_lshlrev_b32 v212, 16, v214\n\t"
    "v_and_b32 v213, 0xffff0000, v214\n\t"
    "v_mfma_f32_16x16x32_bf16 v[64:67], v[140:143], v[184:187], v[64:67]\n\t"
    "v_lshlrev_b32 v214, 16, v215\n\t"
    "v_and_b32 v215, 0xffff0000, v215\n\t"
    "v_mfma_f32_16x16x32_bf16 v[64:67], v[144:147], v[180:183], v[64:67]\n\t"
    "v_mul_f32 v212, s86, v212\n\t"
    "v_mul_f32 v213, s86, v213\n\t"
    "v_mfma_f32_16x16x32_bf16 v[64:67], v[148:151], v[176:179], v[64:67]\n\t"
    "v_mul_f32 v214, s86, v214\n\t"
    "v_mul_f32 v215, s86, v215\n\t"
    "v_mfma_f32_16x16x32_bf16 v[64:67], v[152:155], v[172:175], v[64:67]\n\t"
    "v_lshlrev_b32 v216, 16, v218\n\t"
    "v_and_b32 v217, 0xffff0000, v218\n\t"
    "v_mfma_f32_16x16x32_bf16 v[64:67], v[156:159], v[168:171], v[64:67]\n\t"
    "v_lshlrev_b32 v218, 16, v219\n\t"
    "v_and_b32 v219, 0xffff0000, v219\n\t"
    "v_mfma_f32_16x16x32_bf16 v[64:67], v[160:163], v[164:167], v[64:67]\n\t"
    "ds_read_b64_tr_b16 v[132:133], v232 offset:200\n\t"
    "v_mul_f32 v216, s87, v216\n\t"
    "v_mfma_f32_16x16x32_bf16 v[36:39], v[68:71], v[100:103], v[36:39]\n\t"
    "ds_read_b64_tr_b16 v[134:135], v232 offset:192\n\t"
    "v_mul_f32 v217, s87, v217\n\t"
    "v_mfma_f32_16x16x32_bf16 v[40:43], v[68:71], v[104:107], v[40:43]\n\t"
    "s_waitcnt lgkmcnt(6)\n\t"
    "ds_read_b128 v[164:167], v233 offset:0\n\t"
    "v_mfma_f32_16x16x32_bf16 v[40:43], v[72:75], v[100:103], v[40:43]\n\t"
    "ds_read_b64_tr_b16 v[136:137], v232 offset:712\n\t"
    "v_mul_f32 v218, s87, v218\n\t"
    "v_mfma_f32_16x16x32_bf16 v[44:47], v[68:71], v[108:111], v[44:47]\n\t"
    "ds_read_b64_tr_b16 v[138:139], v232 offset:704\n\t"
    "v_mul_f32 v219, s87, v219\n\t"
    "v_mfma_f32_16x16x32_bf16 v[44:47], v[72:75], v[104:107], v[44:47]\n\t"
    "ds_read_b128 v[168:171], v233 offset:512\n\t"
    "s_waitcnt vmcnt(4)\n\t"
    "buffer_store_dwordx2 v[228:229], v239, %[sw], 0 offen sc1\n\t"
    "v_mfma_f32_16x16x32_bf16 v[44:47], v[76:79], v[100:103], v[44:47]\n\t"
    "ds_read_b64_tr_b16 v[140:141], v232 offset:1224\n\t"
    "s_add_u32 s98, s96, 79872\n\t"
    "s_addc_u32 s99, s97, 0\n\t"
    "v_mfma_f32_16x16x32_bf16 v[48:51], v[68:71], v[112:115], v[48:51]\n\t"
    "ds_read_b64_tr_b16 v[142:143], v232 offset:1216\n\t"
    "global_load_lds_dwordx4 v2, s[98:99] offset:2048\n\t"
    "v_mfma_f32_16x16x32_bf16 v[48:51], v[72:75], v[108:111], v[48:51]\n\t"
    "ds_read_b128 v[172:175], v233 offset:1024\n\t"
    "v_mfma_f32_16x16x32_bf16 v[48:51], v[76:79], v[104:107], v[48:51]\n\t"
    "ds_read_b64_tr_b16 v[144:145], v232 offset:1736\n\t"
    "v_mfma_f32_16x16x32_bf16 v[48:51], v[80:83], v[100:103], v[48:51]\n\t"
    "s_waitcnt lgkmcnt(7)\n\t"
    "ds_read_b64_tr_b16 v[146:147], v232 offset:1728\n\t"
    "v_mfma_f32_16x16x32_bf16 v[52:55], v[68:71], v[116:119], v[52:55]\n\t"
    "ds_read_b128 v[176:179], v233 offset:1536\n\t"
    "v_mfma_f32_16x16x32_bf16 v[52:55], v[72:75], v[112:115], v[52:55]\n\t"
    "ds_read_b64_tr_b16 v[148:149], v232 offset:2248\n\t"
    "v_mfma_f32_16x16x32_bf16 v[52:55], v[76:79], v[108:111], v[52:55]\n\t"
    "ds_read_b64_tr_b16 v[150:151], v232 offset:2240\n\t"
    "v_mfma_f32_16x16x32_bf16 v[52:55], v[80:83], v[104:107], v[52:55]\n\t"
    "ds_read_b128 v[180:183], v233 offset:2048\n\t"
    "global_load_lds_dwordx4 v226, s[98:99] offset:3072\n\t"
    "v_mfma_f32_16x16x32_bf16 v[52:55], v[84:87], v[100:103], v[52:55]\n\t"
    "ds_read_b64_tr_b16 v[152:153], v232 offset:2760\n\t"
    "v_mfma_f32_16x16x32_bf16 v[56:59], v[68:71], v[120:123], v[56:59]\n\t"
    "ds_read_b64_tr_b16 v[154:155], v232 offset:2752\n\t"
    "v_mfma_f32_16x16x32_bf16 v[56:59], v[72:75], v[116:119], v[56:59]\n\t"
    "s_waitcnt lgkmcnt(6)\n\t"
    "ds_read_b128 v[184:187], v233 offset:2560\n\t"
    "v_mfma_f32_16x16x32_bf16 v[56:59], v[76:79], v[112:115], v[56:59]\n\t"
    "ds_read_b64_tr_b16 v[156:157], v232 offset:3272\n\t"
    "v_mfma_f32_16x16x32_bf16 v[56:59], v[80:83], v[108:111], v[56:59]\n\t"
    "ds_read_b64_tr_b16 v[158:159], v232 offset:3264\n\t"
    "v_mfma_f32_16x16x32_bf16 v[56:59], v[84:87], v[104:107], v[56:59]\n\t"
    "ds_read_b128 v[188:191], v233 offset:3072\n\t"
    "v_mfma_f32_16x16x32_bf16 v[56:59], v[88:91], v[100:103], v[56:59]\n\t"
    "ds_read_b64_tr_b16 v[160:161], v232 offset:3784\n\t"
    "s_add_u32 m0, s89, 4096\n\t"
    "v_mfma_f32_16x16x32_bf16 v[60:63], v[68:71], v[124:127], v[60:63]\n\t"
    "ds_read_b64_tr_b16 v[162:163], v232 offset:3776\n\t"
    "s_add_u32 s98, s96, 114688\n\t"
    "s_addc_u32 s99, s97, 0\n\t"
    "v_mfma_f32_16x16x32_bf16 v[60:63], v[72:75], v[120:123], v[60:63]\n\t"
    "ds_read_b128 v[192:195], v233 offset:3584\n\t"
    "global_load_lds_dwordx4 v2, s[98:99] offset:0\n\t"
    "v_mfma_f32_16x16x32_bf16 v[60:63], v[76:79], v[116:119], v[60:63]\n\t"
    "v_mfma_f32_16x16x32_bf16 v[60:63], v[80:83], v[112:115], v[60:63]\n\t"
    "v_mfma_f32_16x16x32_bf16 v[60:63], v[84:87], v[108:111], v[60:63]\n\t"
    "v_mfma_f32_16x16x32_bf16 v[60:63], v[88:91], v[104:107], v[60:63]\n\t"
    "v_mfma_f32_16x16x32_bf16 v[60:63], v[92:95], v[100:103], v[60:63]\n\t"
    "v_mfma_f32_16x16x32_bf16 v[64:67], v[68:71], v[128:131], v[64:67]\n\t"
    "global_load_lds_dwordx4 v226, s[98:99] offset:1024\n\t"
    "v_mfma_f32_16x16x32_bf16 v[64:67], v[72:75], v[124:127], v[64:67]\n\t"
    "v_mfma_f32_16x16x32_bf16 v[64:67], v[76:79], v[120:123], v[64:67]\n\t"
    "v_mfma_f32_16x16x32_bf16 v[64:67], v[80:83], v[116:119], v[64:67]\n\t"
    "v_mfma_f32_16x16x32_bf16 v[64:67], v[84:87], v[112:115], v[64:67]\n\t"
    "v_mfma_f32_16x16x32_bf16 v[64:67], v[88:91], v[108:111], v[64:67]\n\t"
    "v_mfma_f32_16x16x32_bf16 v[64:67], v[92:95], v[104:107], v[64:67]\n\t"
    "v_mfma_f32_16x16x32_bf16 v[64:67], v[96:99], v[100:103], v[64:67]\n\t"
    "s_add_u32 s98, s96, 194560\n\t"
    "s_addc_u32 s99, s97, 0\n\t"
    "v_mfma_f32_16x16x32_bf16 v[36:39], v[132:135], v[164:167], v[36:39]\n\t"
    "global_load_lds_dwordx4 v2, s[98:99] offset:2048\n\t"
    "v_mfma_f32_16x16x32_bf16 v[40:43], v[132:135], v[168:171], v[40:43]\n\t"
    "v_mfma_f32_16x16x32_bf16 v[40:43], v[136:139], v[164:167], v[40:43]\n\t"
    "v_mfma_f32_16x16x32_bf16 v[44:47], v[132:135], v[172:175], v[44:47]\n\t"
    "v_mfma_f32_16x16x32_bf16 v[44:47], v[136:139], v[168:171], v[44:47]\n\t"
    "v_mfma_f32_16x16x32_bf16 v[44:47], v[140:143], v[164:167], v[44:47]\n\t"
    "s_waitcnt lgkmcnt(0)\n\t"
    "s_barrier\n\t"
    "v_mfma_f32_16x16x32_bf16 v[48:51], v[132:135], v[176:179], v[48:51]\n\t"
    "v_mfma_f32_16x16x32_bf16 v[48:51], v[136:139], v[172:175], v[48:51]\n\t"
    "global_load_lds_dwordx4 v226, s[98:99] offset:3072\n\t"
    "v_mfma_f32_16x16x32_bf16 v[48:51], v[140:143], v[168:171], v[48:51]\n\t"
    "ds_write_b128 v234, v[36:39] offset:0\n\t"
    "v_mfma_f32_16x16x32_bf16 v[48:51], v[144:147], v[164:167], v[48:51]\n\t"
    "v_mfma_f32_16x16x32_bf16 v[52:55], v[132:135], v[180:183], v[52:55]\n\t"
    "ds_write_b128 v234, v[40:43] offset:1024\n\t"
    "v_mfma_f32_16x16x32_bf16 v[52:55], v[136:139], v[176:179], v[52:55]\n\t"
    "v_mfma_f32_16x16x32_bf16 v[52:55], v[140:143], v[172:175], v[52:55]\n\t"
    "v_mfma_f32_16x16x32_bf16 v[52:55], v[144:147], v[168:171], v[52:55]\n\t"
    "ds_write_b128 v234, v[44:47] offset:2048\n\t"
    "v_mfma_f32_16x16x32_bf16 v[52:55], v[148:151], v[164:167], v[52:55]\n\t"
    "s_add_u32 m0, s89, 8192\n\t"
    "v_mfma_f32_16x16x32_bf16 v[56:59], v[132:135], v[184:187], v[56:59]\n\t"
    "s_add_u32 s98, s96, 229376\n\t"
    "s_addc_u32 s99, s97, 0\n\t"
    "v_mfma_f32_16x16x32_bf16 v[56:59], v[136:139], v[180:183], v[56:59]\n\t"
    "global_load_lds_dwordx4 v2, s[98:99] offset:0\n\t"
    "v_mfma_f32_16x16x32_bf16 v[56:59], v[140:143], v[176:179], v[56:59]\n\t"
    "ds_write_b128 v234, v[48:51] offset:3072\n\t"
    "v_mfma_f32_16x16x32_bf16 v[56:59], v[144:147], v[172:175], v[56:59]\n\t"
    "v_mfma_f32_16x16x32_bf16 v[56:59], v[148:151], v[168:171], v[56:59]\n\t"
    "v_mfma_f32_16x16x32_bf16 v[56:59], v[152:155], v[164:167], v[56:59]\n\t"
    "v_mfma_f32_16x16x32_bf16 v[60:63], v[132:135], v[188:191], v[60:63]\n\t"
    "v_mfma_f32_16x16x32_bf16 v[60:63], v[136:139], v[184:187], v[60:63]\n\t"
    "global_load_lds_dwordx4 v226, s[98:99] offset:1024\n\t"
    "ds_write_b128 v234, v[52:55] offset:4096\n\t"
    "v_mfma_f32_16x16x32_bf16 v[60:63], v[140:143], v[180:183], v[60:63]\n\t"
    "v_mfma_f32_16x16x32_bf16 v[60:63], v[144:147], v[176:179], v[60:63]\n\t"
    "v_mfma_f32_16x16x32_bf16 v[60:63], v[148:151], v[172:175], v[60:63]\n\t"
    "v_mfma_f32_16x16x32_bf16 v[60:63], v[152:155], v[168:171], v[60:63]\n\t"
    "v_mfma_f32_16x16x32_bf16 v[60:63], v[156:159], v[164:167], v[60:63]\n\t"
    "v_mfma_f32_16x16x32_bf16 v[64:67], v[132:135], v[192:195], v[64:67]\n\t"
    "ds_write_b128 v234, v[56:59] offset:5120\n\t"
    "v_mfma_f32_16x16x32_bf16 v[64:67], v[136:139], v[188:191], v[64:67]\n\t"
    "s_add_u32 s98, s96, 243712\n\t"
    "s_addc_u32 s99, s97, 0\n\t"
    "v_mfma_f32_16x16x32_bf16 v[64:67], v[140:143], v[184:187], v[64:67]\n\t"
    "global_load_lds_dwordx4 v2, s[98:99] offset:2048\n\t"
    "v_mfma_f32_16x16x32_bf16 v[64:67], v[144:147], v[180:183], v[64:67]\n\t"
    "v_mfma_f32_16x16x32_bf16 v[64:67], v[148:151], v[176:179], v[64:67]\n\t"
    "v_mfma_f32_16x16x32_bf16 v[64:67], v[152:155], v[172:175], v[64:67]\n\t"
    "v_mfma_f32_16x16x32_bf16 v[64:67], v[156:159], v[168:171], v[64:67]\n\t"
    "v_mfma_f32_16x16x32_bf16 v[64:67], v[160:163], v[164:167], v[64:67]\n\t"
    "ds_write_b128 v234, v[60:63] offset:6144\n\t"
    "s_nop 6\n\t"
    "ds_write_b128 v234, v[64:67] offset:7168\n\t"
    "global_load_lds_dwordx4 v226, s[98:99] offset:3072\n\t"
    "s_add_u32 m0, s89, 12288\n\t"
    "s_add_u32 s98, s96, 344064\n\t"
    "s_addc_u32 s99, s97, 0\n\t"
    "global_load_lds_dwordx4 v2, s[98:99] offset:0\n\t"
    "global_load_lds_dwordx4 v226, s[98:99] offset:1024\n\t"
    "s_add_u32 s98, s96, 358400\n\t"
    "s_addc_u32 s99, s97, 0\n\t"
    "global_load_lds_dwordx4 v2, s[98:99] offset:2048\n\t"
    "global_load_lds_dwordx4 v226, s[98:99] offset:3072\n\t"
    "s_add_u32 m0, s89, 16384\n\t"
    "s_add_u32 s98, s96, 98304\n\t"
    "s_addc_u32 s99, s97, 0\n\t"
    "global_load_lds_dwordx4 v2, s[98:99] offset:0\n\t"
    "global_load_lds_dwordx4 v226, s[98:99] offset:1024\n\t"
    "s_add_u32 s98, s96, 210944\n\t"
    "s_addc_u32 s99, s97, 0\n\t"
    "global_load_lds_dwordx4 v2, s[98:99] offset:2048\n\t"
    "global_load_lds_dwordx4 v226, s[98:99] offset:3072\n\t"
    "s_waitcnt lgkmcnt(0)\n\t"
    "s_barrier\n\t"
    "ds_read_b128 v[36:39], v12 offset:0\n\t"
    "ds_read_b128 v[40:43], v12 offset:8192\n\t"
    "ds_read_b128 v[44:47], v12 offset:16384\n\t"
    "ds_read_b128 v[48:51], v12 offset:24576\n\t"
    "ds_read_b128 v[52:55], v12 offset:1024\n\t"
    "ds_read_b128 v[56:59], v12 offset:9216\n\t"
    "ds_read_b128 v[60:63], v12 offset:17408\n\t"
    "ds_read_b128 v[64:67], v12 offset:25600\n\t"
    "s_waitcnt lgkmcnt(6)\n\t"
    "v_pk_add_f32 v[36:37], v[36:37], v[40:41]\n\t"
    "v_pk_add_f32 v[38:39], v[38:39], v[42:43]\n\t"
    "v_pk_add_f32 v[36:37], v[36:37], v[204:205]\n\t"
    "v_pk_add_f32 v[38:39], v[38:39], v[206:207]\n\t"
    "s_waitcnt lgkmcnt(4)\n\t"
    "v_pk_add_f32 v[44:45], v[44:45], v[48:49]\n\t"
    "v_pk_add_f32 v[46:47], v[46:47], v[50:51]\n\t"
    "v_pk_add_f32 v[44:45], v[44:45], v[208:209]\n\t"
    "v_pk_add_f32 v[46:47], v[46:47], v[210:211]\n\t"
    "v_cvt_pk_bf16_f32 v40, v36, v44\n\t"
    "v_cvt_pk_bf16_f32 v41, v37, v45\n\t"
    "v_cvt_pk_bf16_f32 v42, v38, v46\n\t"
    "v_cvt_pk_bf16_f32 v43, v39, v47\n\t"
    "buffer_store_dwordx4 v[40:43], v14, %[sw], 0 offen sc1\n\t"
    "s_waitcnt lgkmcnt(2)\n\t"
    "v_pk_add_f32 v[52:53], v[52:53], v[56:57]\n\t"
    "v_pk_add_f32 v[54:55], v[54:55], v[58:59]\n\t"
    "v_pk_add_f32 v[52:53], v[52:53], v[212:213]\n\t"
    "v_pk_add_f32 v[54:55], v[54:55], v[214:215]\n\t"
    "s_waitcnt lgkmcnt(0)\n\t"
    "v_pk_add_f32 v[60:61], v[60:61], v[64:65]\n\t"
    "v_pk_add_f32 v[62:63], v[62:63], v[66:67]\n\t"
    "v_pk_add_f32 v[60:61], v[60:61], v[216:217]\n\t"
    "v_pk_add_f32 v[62:63], v[62:63], v[218:219]\n\t"
    "v_cvt_pk_bf16_f32 v56, v52, v60\n\t"
    "v_cvt_pk_bf16_f32 v57, v53, v61\n\t"
    "v_cvt_pk_bf16_f32 v58, v54, v62\n\t"
    "v_cvt_pk_bf16_f32 v59, v55, v63\n\t"
    "s_mov_b32 s84, 262144\n\t"
    "buffer_store_dwordx4 v[56:59], v14, %[sw], s84 offen sc1\n\t"
    "buffer_load_dwordx4 v[68:71], v252, %[sw], 0 offen sc1\n\t"
    "s_mul_i32 s86, s60, 24576\n\t"
    "v_xor_b32 v220, 0, v1\n\t"
    "v_add_u32 v220, s86, v220\n\t"
    "v_xor_b32 v221, 32, v1\n\t"
    "v_add_u32 v221, s86, v221\n\t"
    "v_xor_b32 v222, 64, v1\n\t"
    "v_add_u32 v222, s86, v222\n\t"
    "v_xor_b32 v223, 96, v1\n\t"
    "v_add_u32 v223, s86, v223\n\t"
    "s_waitcnt vmcnt(3)\n\t"
    "ds_read_b64_tr_b16 a[0:1], v220 offset:0\n\t"
    "ds_read_b64_tr_b16 a[2:3], v220 offset:2048\n\t"
    "ds_read_b64_tr_b16 a[4:5], v221 offset:0\n\t"
    "ds_read_b64_tr_b16 a[6:7], v221 offset:2048\n\t"
    "ds_read_b64_tr_b16 a[8:9], v222 offset:0\n\t"
    "ds_read_b64_tr_b16 a[10:11], v222 offset:2048\n\t"
    "ds_read_b64_tr_b16 a[12:13], v223 offset:0\n\t"
    "ds_read_b64_tr_b16 a[14:15], v223 offset:2048\n\t"
    "ds_read_b64_tr_b16 a[16:17], v220 offset:4096\n\t"
    "ds_read_b64_tr_b16 a[18:19], v220 offset:6144\n\t"
    "ds_read_b64_tr_b16 a[20:21], v221 offset:4096\n\t"
    "ds_read_b64_tr_b16 a[22:23], v221 offset:6144\n\t"
    "ds_read_b64_tr_b16 a[24:25], v222 offset:4096\n\t"
    "ds_read_b64_tr_b16 a[26:27], v222 offset:6144\n\t"
    "ds_read_b64_tr_b16 a[28:29], v223 offset:4096\n\t"
    "s_waitcnt lgkmcnt(0)\n\t"
    "ds_read_b64_tr_b16 a[30:31], v223 offset:6144\n\t"
    "ds_read_b64_tr_b16 a[32:33], v220 offset:8192\n\t"
    "ds_read_b64_tr_b16 a[34:35], v220 offset:10240\n\t"
    "ds_read_b64_tr_b16 a[36:37], v221 offset:8192\n\t"
    "ds_read_b64_tr_b16 a[38:39], v221 offset:10240\n\t"
    "ds_read_b64_tr_b16 a[40:41], v222 offset:8192\n\t"
    "ds_read_b64_tr_b16 a[42:43], v222 offset:10240\n\t"
    "ds_read_b64_tr_b16 a[44:45], v223 offset:8192\n\t"
    "ds_read_b64_tr_b16 a[46:47], v223 offset:10240\n\t"
    "ds_read_b64_tr_b16 a[48:49], v220 offset:12288\n\t"
    "ds_read_b64_tr_b16 a[50:51], v220 offset:14336\n\t"
    "ds_read_b64_tr_b16 a[52:53], v221 offset:12288\n\t"
    "ds_read_b64_tr_b16 a[54:55], v221 offset:14336\n\t"
    "ds_read_b64_tr_b16 a[56:57], v222 offset:12288\n\t"
    "ds_read_b64_tr_b16 a[58:59], v222 offset:14336\n\t"
    "s_waitcnt lgkmcnt(0)\n\t"
    "ds_read_b64_tr_b16 a[60:61], v223 offset:12288\n\t"
    "ds_read_b64_tr_b16 a[62:63], v223 offset:14336\n\t"
    "ds_read_b64_tr_b16 a[64:65], v220 offset:16384\n\t"
    "ds_read_b64_tr_b16 a[66:67], v220 offset:18432\n\t"
    "ds_read_b64_tr_b16 a[68:69], v221 offset:16384\n\t"
    "ds_read_b64_tr_b16 a[70:71], v221 offset:18432\n\t"
    "ds_read_b64_tr_b16 a[72:73], v222 offset:16384\n\t"
    "ds_read_b64_tr_b16 a[74:75], v222 offset:18432\n\t"
    "ds_read_b64_tr_b16 a[76:77], v223 offset:16384\n\t"
    "ds_read_b64_tr_b16 a[78:79], v223 offset:18432\n\t"
    "v_lshlrev_b32 v204, 16, v196\n\t"
    "v_and_b32 v205, 0xffff0000, v196\n\t"
    "v_lshlrev_b32 v206, 16, v197\n\t"
    "v_and_b32 v207, 0xffff0000, v197\n\t"
    "v_lshlrev_b32 v208, 16, v198\n\t"
    "v_and_b32 v209, 0xffff0000, v198\n\t"
    "v_lshlrev_b32 v210, 16, v199\n\t"
    "v_and_b32 v211, 0xffff0000, v199\n\t"
    "v_lshlrev_b32 v212, 16, v200\n\t"
    "v_and_b32 v213, 0xffff0000, v200\n\t"
    "v_lshlrev_b32 v214, 16, v201\n\t"
    "v_and_b32 v215, 0xffff0000, v201\n\t"
    "v_lshlrev_b32 v216, 16, v202\n\t"
    "v_and_b32 v217, 0xffff0000, v202\n\t"
    "v_lshlrev_b32 v218, 16, v203\n\t"
    "v_and_b32 v219, 0xffff0000, v203\n\t"
    "v_pk_add_f32 v[72:73], v[204:205], v[206:207]\n\t"
    "v_pk_mul_f32 v[74:75], v[204:205], v[204:205]\n\t"
    "v_pk_fma_f32 v[74:75], v[206:207], v[206:207], v[74:75]\n\t"
    "v_pk_fma_f32 v[74:75], v[208:209], v[208:209], v[74:75]\n\t"
    "v_pk_add_f32 v[72:73], v[72:73], v[208:209]\n\t"
    "v_pk_fma_f32 v[74:75], v[210:211], v[210:211], v[74:75]\n\t"
    "v_pk_add_f32 v[72:73], v[72:73], v[210:211]\n\t"
    "v_pk_fma_f32 v[74:75], v[212:213], v[212:213], v[74:75]\n\t"
    "v_pk_add_f32 v[72:73], v[72:73], v[212:213]\n\t"
    "v_pk_fma_f32 v[74:75], v[214:215], v[214:215], v[74:75]\n\t"
    "v_pk_add_f32 v[72:73], v[72:73], v[214:215]\n\t"
    "v_pk_fma_f32 v[74:75], v[216:217], v[216:217], v[74:75]\n\t"
    "v_pk_add_f32 v[72:73], v[72:73], v[216:217]\n\t"
    "v_pk_fma_f32 v[74:75], v[218:219], v[218:219], v[74:75]\n\t"
    "v_pk_add_f32 v[72:73], v[72:73], v[218:219]\n\t"
    "v_add_f32 v72, v72, v73\n\t"
    "v_add_f32 v74, v74, v75\n\t"
    "s_nop 1\n\t"
    "v_permlane32_swap_b32 v72, v74\n\t"
    "v_add_f32 v72, v72, v74\n\t"
    "v_mov_b32 v73, v72\n\t"
    "s_nop 1\n\t"
    "v_permlane16_swap_b32 v72, v73\n\t"
    "v_add_f32 v72, v72, v73\n\t"
    "s_nop 1\n\t"
    "v_add_f32_dpp v72, v72, v72 row_ror:8 row_mask:0xf bank_mask:0xf\n\t"
    "ds_write_b32 v3, v72\n\t"
    "v_mov_b32 v84, 1\n\t"
    "v_mov_b32 v85, 0\n\t"
    "s_waitcnt vmcnt(0)\n\t"
    "v_cmp_ne_u64 vcc, v[68:69], s[56:57]\n\t"
    "v_cmp_ne_u64 s[86:87], v[70:71], s[56:57]\n\t"
    "s_or_b64 vcc, vcc, s[86:87]\n\t"
    "s_cmp_eq_u64 vcc, 0\n\t"
    "s_cbranch_scc1 41f\n\t"
    "s_mov_b32 s88, 0x40000\n\t"
    "40:\n\t"
    "buffer_load_dwordx4 v[68:71], v252, %[sw], 0 offen sc1\n\t"
    "s_waitcnt vmcnt(0)\n\t"
    "v_cmp_ne_u64 vcc, v[68:69], s[56:57]\n\t"
    "v_cmp_ne_u64 s[86:87], v[70:71], s[56:57]\n\t"
    "s_or_b64 vcc, vcc, s[86:87]\n\t"
    "s_cmp_eq_u64 vcc, 0\n\t"
    "s_cbranch_scc1 41f\n\t"
    "s_sub_u32 s88, s88, 1\n\t"
    "s_cmp_lg_u32 s88, 0\n\t"
    "s_cbranch_scc1 40b\n\t"
    "41:\n\t"
    "s_waitcnt lgkmcnt(0)\n\t"
    "s_barrier\n\t"
    "buffer_atomic_add_x2 v[84:85], v15, %[sw], 0 offen\n\t"
    "s_mov_b32 s86, 473088\n\t"
    "buffer_load_dwordx4 v[36:39], v247, %[sw], s86 offen sc1\n\t"
    "s_mov_b32 s86, 485376\n\t"
    "buffer_load_dwordx4 v[40:43], v247, %[sw], s86 offen sc1\n\t"
    "s_mov_b32 s86, 477184\n\t"
    "buffer_load_dwordx4 v[44:47], v247, %[sw], s86 offen sc1\n\t"
    "s_mov_b32 s86, 489472\n\t"
    "buffer_load_dwordx4 v[48:51], v247, %[sw], s86 offen sc1\n\t"
    "s_mov_b32 s86, 481280\n\t"
    "buffer_load_dwordx4 v[52:55], v247, %[sw], s86 offen sc1\n\t"
    "s_mov_b32 s86, 493568\n\t"
    "buffer_load_dwordx4 v[56:59], v247, %[sw], s86 offen sc1\n\t"
    "s_add_u32 m0, s67, 8192\n\t"
    "s_add_u32 s85, s65, 327680\n\t"
    "buffer_load_dwordx4 v2, %[sd], s85 offen offset:0 lds\n\t"
    "s_add_u32 s85, s65, 334848\n\t"
    "buffer_load_dwordx4 v2, %[sd], s85 offen offset:1024 lds\n\t"
    "s_add_u32 s85, s65, 374784\n\t"
    "buffer_load_dwordx4 v2, %[sd], s85 offen offset:2048 lds\n\t"
    "s_add_u32 s85, s65, 381952\n\t"
    "buffer_load_dwordx4 v2, %[sd], s85 offen offset:3072 lds\n\t"
    "s_add_u32 m0, s67, 0\n\t"
    "s_add_u32 s85, s65, 0\n\t"
    "buffer_load_dwordx4 v2, %[sd], s85 offen offset:0 lds\n\t"
    "s_add_u32 s85, s65, 7168\n\t"
    "buffer_load_dwordx4 v2, %[sd], s85 offen offset:1024 lds\n\t"
    "s_add_u32 s85, s65, 14336\n\t"
    "buffer_load_dwordx4 v2, %[sd], s85 offen offset:2048 lds\n\t"
    "s_add_u32 s85, s65, 21504\n\t"
    "buffer_load_dwordx4 v2, %[sd], s85 offen offset:3072 lds\n\t"
    "s_add_u32 m0, s67, 4096\n\t"
    "s_add_u32 s85, s65, 131072\n\t"
    "buffer_load_dwordx4 v2, %[sd], s85 offen offset:0 lds\n\t"
    "s_add_u32 s85, s65, 138240\n\t"
    "buffer_load_dwordx4 v2, %[sd], s85 offen offset:1024 lds\n\t"
    "s_add_u32 s85, s65, 145408\n\t"
    "buffer_load_dwordx4 v2, %[sd], s85 offen offset:2048 lds\n\t"
    "s_add_u32 s85, s65, 152576\n\t"
    "buffer_load_dwordx4 v2, %[sd], s85 offen offset:3072 lds\n\t"
    "ds_read_b128 v[76:79], v17\n\t"
    "ds_read_b128 v[80:83], v17 offset:128\n\t"
    "v_xor_b32 v220, 0, v1\n\t"
    "v_add_u32 v220, s67, v220\n\t"
    "v_xor_b32 v221, 32, v1\n\t"
    "v_add_u32 v221, s67, v221\n\t"
    "v_xor_b32 v222, 64, v1\n\t"
    "v_add_u32 v222, s67, v222\n\t"
    "v_xor_b32 v223, 96, v1\n\t"
    "v_add_u32 v223, s67, v223\n\t"
    "s_waitcnt lgkmcnt(0)\n\t"
    "v_pk_add_f32 v[76:77], v[76:77], v[78:79]\n\t"
    "v_pk_add_f32 v[80:81], v[80:81], v[82:83]\n\t"
    "v_add_f32 v76, v76, v77\n\t"
    "v_add_f32 v80, v80, v81\n\t"
    "v_mul_f32 v76, 0x3b000000, v76\n\t"
    "v_mul_f32 v80, 0x3b000000, v80\n\t"
    "v_fma_f32 v80, -v76, v76, v80\n\t"
    "v_max_f32 v80, 0, v80\n\t"
    "v_cvt_pk_bf16_f32 v80, v80, v80\n\t"
    "v_and_b32 v80, 0xffff0000, v80\n\t"
    "v_cvt_pk_bf16_f32 v76, v76, v76\n\t"
    "v_and_b32 v76, 0xffff0000, v76\n\t"
    "v_add_f32 v80, 0x3727c5ac, v80\n\t"
    "v_cvt_pk_bf16_f32 v80, v80, v80\n\t"
    "v_and_b32 v80, 0xffff0000, v80\n\t"
    "v_sqrt_f32 v80, v80\n\t"
    "s_nop 0\n\t"
    "v_cvt_pk_bf16_f32 v80, v80, v80\n\t"
    "v_and_b32 v80, 0xffff0000, v80\n\t"
    "v_rcp_f32 v80, v80\n\t"
    "s_nop 0\n\t"
    "v_mul_f32 v76, -v76, v80\n\t"
    "s_cmp_lt_u32 s60, 2\n\t"
    "s_cselect_b32 s86, 0, 1.0\n\t"
    "s_cselect_b32 s87, 1.0, 0\n\t"
    "s_waitcnt vmcnt(16)\n\t"
    "v_pk_add_f32 v[36:37], v[36:37], v[40:41]\n\t"
    "v_pk_add_f32 v[38:39], v[38:39], v[42:43]\n\t"
    "v_cvt_pk_bf16_f32 v40, v36, v37\n\t"
    "v_cvt_pk_bf16_f32 v41, v38, v39\n\t"
    "v_lshlrev_b32 v36, 16, v40\n\t"
    "v_and_b32 v37, 0xffff0000, v40\n\t"
    "v_lshlrev_b32 v38, 16, v41\n\t"
    "v_and_b32 v39, 0xffff0000, v41\n\t"
    "v_add_f32 v36, s86, v36\n\t"
    "v_add_f32 v37, s86, v37\n\t"
    "v_add_f32 v38, s86, v38\n\t"
    "v_add_f32 v39, s86, v39\n\t"
    "v_cvt_pk_bf16_f32 v40, v36, v37\n\t"
    "v_cvt_pk_bf16_f32 v41, v38, v39\n\t"
    "ds_write_b64 v19, v[40:41] offset:0\n\t"
    "s_waitcnt vmcnt(14)\n\t"
    "v_pk_add_f32 v[44:45], v[44:45], v[48:49]\n\t"
    "v_pk_add_f32 v[46:47], v[46:47], v[50:51]\n\t"
    "v_cvt_pk_bf16_f32 v48, v44, v45\n\t"
    "v_cvt_pk_bf16_f32 v49, v46, v47\n\t"
    "ds_write_b64 v19, v[48:49] offset:2048\n\t"
    "s_waitcnt vmcnt(12)\n\t"
    "v_pk_add_f32 v[52:53], v[52:53], v[56:57]\n\t"
    "v_pk_add_f32 v[54:55], v[54:55], v[58:59]\n\t"
    "v_cvt_pk_bf16_f32 v56, v52, v53\n\t"
    "v_cvt_pk_bf16_f32 v57, v54, v55\n\t"
    "v_lshlrev_b32 v52, 16, v56\n\t"
    "v_and_b32 v53, 0xffff0000, v56\n\t"
    "v_lshlrev_b32 v54, 16, v57\n\t"
    "v_and_b32 v55, 0xffff0000, v57\n\t"
    "v_add_f32 v52, s87, v52\n\t"
    "v_add_f32 v53, s87, v53\n\t"
    "v_add_f32 v54, s87, v54\n\t"
    "v_add_f32 v55, s87, v55\n\t"
    "v_cvt_pk_bf16_f32 v56, v52, v53\n\t"
    "v_cvt_pk_bf16_f32 v57, v54, v55\n\t"
    "ds_write_b64 v19, v[56:57] offset:4096\n\t"
    "buffer_load_dwordx2 v[92:93], v16, %[sw], 0 offen sc1\n\t"
    "s_waitcnt lgkmcnt(0)\n\t"
    "s_barrier\n\t"
    "ds_read_b128 v[36:39], v24 offset:0\n\t"
    "ds_read_b128 v[40:43], v24 offset:32\n\t"
    "ds_read_b128 v[44:47], v24 offset:1024\n\t"
    "ds_read_b128 v[48:51], v24 offset:1056\n\t"
    "ds_read_b128 v[52:55], v24 offset:2048\n\t"
    "ds_read_b128 v[56:59], v24 offset:2080\n\t"
    "ds_read_b128 v[60:63], v24 offset:3072\n\t"
    "ds_read_b128 v[64:67], v24 offset:3104\n\t"
    "ds_read_b128 v[68:71], v24 offset:4096\n\t"
    "ds_read_b128 v[72:75], v24 offset:4128\n\t"
    "ds_read_b32 v34, v30\n\t"
    "v_pk_fma_f32 v[100:101], v[204:205], v[80:81], v[76:77] op_sel_hi:[1,0,0]\n\t"
    "v_pk_fma_f32 v[102:103], v[206:207], v[80:81], v[76:77] op_sel_hi:[1,0,0]\n\t"
    "v_pk_fma_f32 v[104:105], v[208:209], v[80:81], v[76:77] op_sel_hi:[1,0,0]\n\t"
    "v_pk_fma_f32 v[106:107], v[210:211], v[80:81], v[76:77] op_sel_hi:[1,0,0]\n\t"
    "v_pk_fma_f32 v[108:109], v[212:213], v[80:81], v[76:77] op_sel_hi:[1,0,0]\n\t"
    "v_pk_fma_f32 v[110:111], v[214:215], v[80:81], v[76:77] op_sel_hi:[1,0,0]\n\t"
    "v_pk_fma_f32 v[112:113], v[216:217], v[80:81], v[76:77] op_sel_hi:[1,0,0]\n\t"
    "v_pk_fma_f32 v[114:115], v[218:219], v[80:81], v[76:77] op_sel_hi:[1,0,0]\n\t"
    "s_waitcnt lgkmcnt(7)\n\t"
    "v_lshlrev_b32 v84, 16, v44\n\t"
    "v_and_b32 v85, 0xffff0000, v44\n\t"
    "v_lshlrev_b32 v86, 16, v36\n\t"
    "v_and_b32 v87, 0xffff0000, v36\n\t"
    "v_pk_fma_f32 v[100:101], v[100:101], v[84:85], v[86:87]\n\t"
    "v_lshlrev_b32 v88, 16, v45\n\t"
    "v_and_b32 v89, 0xffff0000, v45\n\t"
    "v_lshlrev_b32 v90, 16, v37\n\t"
    "v_and_b32 v91, 0xffff0000, v37\n\t"
    "v_pk_fma_f32 v[102:103], v[102:103], v[88:89], v[90:91]\n\t"
    "v_lshlrev_b32 v84, 16, v46\n\t"
    "v_and_b32 v85, 0xffff0000, v46\n\t"
    "v_lshlrev_b32 v86, 16, v38\n\t"
    "v_and_b32 v87, 0xffff0000, v38\n\t"
    "v_pk_fma_f32 v[104:105], v[104:105], v[84:85], v[86:87]\n\t"
    "v_lshlrev_b32 v88, 16, v47\n\t"
    "v_and_b32 v89, 0xffff0000, v47\n\t"
    "v_lshlrev_b32 v90, 16, v39\n\t"
    "v_and_b32 v91, 0xffff0000, v39\n\t"
    "v_pk_fma_f32 v[106:107], v[106:107], v[88:89], v[90:91]\n\t"
    "v_lshlrev_b32 v84, 16, v48\n\t"
    "v_and_b32 v85, 0xffff0000, v48\n\t"
    "v_lshlrev_b32 v86, 16, v40\n\t"
    "v_and_b32 v87, 0xffff0000, v40\n\t"
    "v_pk_fma_f32 v[108:109], v[108:109], v[84:85], v[86:87]\n\t"
    "v_lshlrev_b32 v88, 16, v49\n\t"
    "v_and_b32 v89, 0xffff0000, v49\n\t"
    "v_lshlrev_b32 v90, 16, v41\n\t"
    "v_and_b32 v91, 0xffff0000, v41\n\t"
    "v_pk_fma_f32 v[110:111], v[110:111], v[88:89], v[90:91]\n\t"
    "v_lshlrev_b32 v84, 16, v50\n\t"
    "v_and_b32 v85, 0xffff0000, v50\n\t"
    "v_lshlrev_b32 v86, 16, v42\n\t"
    "v_and_b32 v87, 0xffff0000, v42\n\t"
    "v_pk_fma_f32 v[112:113], v[112:113], v[84:85], v[86:87]\n\t"
    "v_lshlrev_b32 v88, 16, v51\n\t"
    "v_and_b32 v89, 0xffff0000, v51\n\t"
    "v_lshlrev_b32 v90, 16, v43\n\t"
    "v_and_b32 v91, 0xffff0000, v43\n\t"
    "v_pk_fma_f32 v[114:115], v[114:115], v[88:89], v[90:91]\n\t"
    "s_waitcnt lgkmcnt(5)\n\t"
    "v_lshlrev_b32 v84, 16, v52\n\t"
    "v_and_b32 v85, 0xffff0000, v52\n\t"
    "v_pk_mul_f32 v[100:101], v[100:101], v[84:85]\n\t"
    "v_lshlrev_b32 v86, 16, v53\n\t"
    "v_and_b32 v87, 0xffff0000, v53\n\t"
    "v_pk_mul_f32 v[102:103], v[102:103], v[86:87]\n\t"
    "v_lshlrev_b32 v88, 16, v54\n\t"
    "v_and_b32 v89, 0xffff0000, v54\n\t"
    "v_pk_mul_f32 v[104:105], v[104:105], v[88:89]\n\t"
    "v_lshlrev_b32 v90, 16, v55\n\t"
    "v_and_b32 v91, 0xffff0000, v55\n\t"
    "v_pk_mul_f32 v[106:107], v[106:107], v[90:91]\n\t"
    "v_lshlrev_b32 v84, 16, v56\n\t"
    "v_and_b32 v85, 0xffff0000, v56\n\t"
    "v_pk_mul_f32 v[108:109], v[108:109], v[84:85]\n\t"
    "v_lshlrev_b32 v86, 16, v57\n\t"
    "v_and_b32 v87, 0xffff0000, v57\n\t"
    "v_pk_mul_f32 v[110:111], v[110:111], v[86:87]\n\t"
    "v_lshlrev_b32 v88, 16, v58\n\t"
    "v_and_b32 v89, 0xffff0000, v58\n\t"
    "v_pk_mul_f32 v[112:113], v[112:113], v[88:89]\n\t"
    "v_lshlrev_b32 v90, 16, v59\n\t"
    "v_and_b32 v91, 0xffff0000, v59\n\t"
    "v_pk_mul_f32 v[114:115], v[114:115], v[90:91]\n\t"
    "s_waitcnt lgkmcnt(0)\n\t"
    "v_lshlrev_b32 v132, 16, v60\n\t"
    "v_and_b32 v133, 0xffff0000, v60\n\t"
    "v_lshlrev_b32 v116, 16, v68\n\t"
    "v_and_b32 v117, 0xffff0000, v68\n\t"
    "v_lshlrev_b32 v134, 16, v61\n\t"
    "v_and_b32 v135, 0xffff0000, v61\n\t"
    "v_lshlrev_b32 v118, 16, v69\n\t"
    "v_and_b32 v119, 0xffff0000, v69\n\t"
    "v_lshlrev_b32 v136, 16, v62\n\t"
    "v_and_b32 v137, 0xffff0000, v62\n\t"
    "v_lshlrev_b32 v120, 16, v70\n\t"
    "v_and_b32 v121, 0xffff0000, v70\n\t"
    "v_lshlrev_b32 v138, 16, v63\n\t"
    "v_and_b32 v139, 0xffff0000, v63\n\t"
    "v_lshlrev_b32 v122, 16, v71\n\t"
    "v_and_b32 v123, 0xffff0000, v71\n\t"
    "v_lshlrev_b32 v140, 16, v64\n\t"
    "v_and_b32 v141, 0xffff0000, v64\n\t"
    "v_lshlrev_b32 v124, 16, v72\n\t"
    "v_and_b32 v125, 0xffff0000, v72\n\t"
    "v_lshlrev_b32 v142, 16, v65\n\t"
    "v_and_b32 v143, 0xffff0000, v65\n\t"
    "v_lshlrev_b32 v126, 16, v73\n\t"
    "v_and_b32 v127, 0xffff0000, v73\n\t"
    "v_lshlrev_b32 v144, 16, v66\n\t"
    "v_and_b32 v145, 0xffff0000, v66\n\t"
    "v_lshlrev_b32 v128, 16, v74\n\t"
    "v_and_b32 v129, 0xffff0000, v74\n\t"
    "v_lshlrev_b32 v146, 16, v67\n\t"
    "v_and_b32 v147, 0xffff0000, v67\n\t"
    "v_lshlrev_b32 v130, 16, v75\n\t"
    "v_and_b32 v131, 0xffff0000, v75\n\t"
    "s_waitcnt vmcnt(9)\n\t"
    "ds_read_b64_tr_b16 a[80:81], v220 offset:8192\n\t"
    "ds_read_b64_tr_b16 a[82:83], v220 offset:10240\n\t"
    "ds_read_b64_tr_b16 a[84:85], v221 offset:8192\n\t"
    "ds_read_b64_tr_b16 a[86:87], v221 offset:10240\n\t"
    "ds_read_b64_tr_b16 a[88:89], v222 offset:8192\n\t"
    "ds_read_b64_tr_b16 a[90:91], v222 offset:10240\n\t"
    "ds_read_b64_tr_b16 a[92:93], v223 offset:8192\n\t"
    "ds_read_b64_tr_b16 a[94:95], v223 offset:10240\n\t"
    "s_waitcnt lgkmcnt(0)\n\t"
    "s_waitcnt vmcnt(5)\n\t"
    "ds_read_b64_tr_b16 a[96:97], v220 offset:0\n\t"
    "ds_read_b64_tr_b16 a[98:99], v220 offset:2048\n\t"
    "ds_read_b64_tr_b16 a[100:101], v221 offset:0\n\t"
    "ds_read_b64_tr_b16 a[102:103], v221 offset:2048\n\t"
    "ds_read_b64_tr_b16 a[104:105], v222 offset:0\n\t"
    "ds_read_b64_tr_b16 a[106:107], v222 offset:2048\n\t"
    "ds_read_b64_tr_b16 a[108:109], v223 offset:0\n\t"
    "ds_read_b64_tr_b16 a[110:111], v223 offset:2048\n\t"
    "s_waitcnt lgkmcnt(0)\n\t"
    "s_waitcnt vmcnt(1)\n\t"
    "ds_read_b64_tr_b16 a[112:113], v220 offset:4096\n\t"
    "ds_read_b64_tr_b16 a[114:115], v220 offset:6144\n\t"
    "ds_read_b64_tr_b16 a[116:117], v221 offset:4096\n\t"
    "ds_read_b64_tr_b16 a[118:119], v221 offset:6144\n\t"
    "ds_read_b64_tr_b16 a[120:121], v222 offset:4096\n\t"
    "ds_read_b64_tr_b16 a[122:123], v222 offset:6144\n\t"
    "ds_read_b64_tr_b16 a[124:125], v223 offset:4096\n\t"
    "ds_read_b64_tr_b16 a[126:127], v223 offset:6144\n\t"
    "s_waitcnt lgkmcnt(0)\n\t"
    "s_waitcnt vmcnt(0)\n\t"
    "v_cmp_ne_u64 vcc, v[92:93], s[90:91]\n\t"
    "s_cmp_eq_u64 vcc, 0\n\t"
    "s_cbranch_scc1 51f\n\t"
    "buffer_load_dwordx2 v[92:93], v16, %[sw], 0 offen sc1\n\t"
    "s_sleep 3\n\t"
    "buffer_load_dwordx2 v[94:95], v16, %[sw], 0 offen sc1\n\t"
    "s_sleep 3\n\t"
    "s_mov_b32 s88, 0x20000\n\t"
    "50:\n\t"
    "buffer_load_dwordx2 v[96:97], v16, %[sw], 0 offen sc1\n\t"
    "s_waitcnt vmcnt(2)\n\t"
    "v_cmp_ne_u64 vcc, v[92:93], s[90:91]\n\t"
    "s_cmp_eq_u64 vcc, 0\n\t"
    "s_cbranch_scc1 51f\n\t"
    "buffer_load_dwordx2 v[92:93], v16, %[sw], 0 offen sc1\n\t"
    "s_waitcnt vmcnt(2)\n\t"
    "v_cmp_ne_u64 vcc, v[94:95], s[90:91]\n\t"
    "s_cmp_eq_u64 vcc, 0\n\t"
    "s_cbranch_scc1 51f\n\t"
    "buffer_load_dwordx2 v[94:95], v16, %[sw], 0 offen sc1\n\t"
    "s_waitcnt vmcnt(2)\n\t"
    "v_cmp_ne_u64 vcc, v[96:97], s[90:91]\n\t"
    "s_cmp_eq_u64 vcc, 0\n\t"
    "s_cbranch_scc1 51f\n\t"
    "s_sub_u32 s88, s88, 1\n\t"
    "s_cmp_lg_u32 s88, 0\n\t"
    "s_cbranch_scc1 50b\n\t"
    "51:\n\t"
    "buffer_load_dword v148, v25, %[sw], 0 offen offset:0 sc1\n\t"
    "buffer_load_dword v149, v25, %[sw], 0 offen offset:128 sc1\n\t"
    "buffer_load_dword v150, v25, %[sw], 0 offen offset:256 sc1\n\t"
    "buffer_load_dword v151, v25, %[sw], 0 offen offset:384 sc1\n\t"
    "buffer_load_dword v152, v25, %[sw], 0 offen offset:1024 sc1\n\t"
    "buffer_load_dword v153, v25, %[sw], 0 offen offset:1152 sc1\n\t"
    "buffer_load_dword v154, v25, %[sw], 0 offen offset:1280 sc1\n\t"
    "buffer_load_dword v155, v25, %[sw], 0 offen offset:1408 sc1\n\t"
    "s_add_u32 m0, s82, 0\n\t"
    "s_add_u32 s84, s66, 0\n\t"
    "buffer_load_dwordx4 v0, %[sw], s84 offen offset:0 sc1 lds\n\t"
    "buffer_load_dwordx4 v0, %[sw], s84 offen offset:1024 sc1 lds\n\t"
    "buffer_load_dwordx4 v0, %[sw], s84 offen offset:2048 sc1 lds\n\t"
    "s_add_u32 m0, s82, 49152\n\t"
    "s_add_u32 s84, s66, 49152\n\t"
    "buffer_load_dwordx4 v0, %[sw], s84 offen offset:0 sc1 lds\n\t"
    "buffer_load_dwordx4 v0, %[sw], s84 offen offset:1024 sc1 lds\n\t"
    "buffer_load_dwordx4 v0, %[sw], s84 offen offset:2048 sc1 lds\n\t"
    "s_waitcnt vmcnt(13)\n\t"
    "v_lshlrev_b32 v156, 16, v148\n\t"
    "v_and_b32 v157, 0xffff0000, v148\n\t"
    "v_pk_fma_f32 v[156:157], v[100:101], v[156:157], v[204:205]\n\t"
    "s_waitcnt vmcnt(12)\n\t"
    "v_lshlrev_b32 v158, 16, v149\n\t"
    "v_and_b32 v159, 0xffff0000, v149\n\t"
    "v_pk_fma_f32 v[158:159], v[102:103], v[158:159], v[206:207]\n\t"
    "v_cvt_pk_bf16_f32 v172, v156, v157\n\t"
    "s_waitcnt vmcnt(11)\n\t"
    "v_lshlrev_b32 v160, 16, v150\n\t"
    "v_and_b32 v161, 0xffff0000, v150\n\t"
    "v_pk_fma_f32 v[160:161], v[104:105], v[160:161], v[208:209]\n\t"
    "v_cvt_pk_bf16_f32 v173, v158, v159\n\t"
    "s_waitcnt vmcnt(10)\n\t"
    "v_lshlrev_b32 v162, 16, v151\n\t"
    "v_and_b32 v163, 0xffff0000, v151\n\t"
    "v_pk_fma_f32 v[162:163], v[106:107], v[162:163], v[210:211]\n\t"
    "v_cvt_pk_bf16_f32 v176, v160, v161\n\t"
    "s_waitcnt vmcnt(9)\n\t"
    "v_lshlrev_b32 v164, 16, v152\n\t"
    "v_and_b32 v165, 0xffff0000, v152\n\t"
    "v_pk_fma_f32 v[164:165], v[108:109], v[164:165], v[212:213]\n\t"
    "v_cvt_pk_bf16_f32 v177, v162, v163\n\t"
    "s_waitcnt vmcnt(8)\n\t"
    "v_lshlrev_b32 v166, 16, v153\n\t"
    "v_and_b32 v167, 0xffff0000, v153\n\t"
    "v_pk_fma_f32 v[166:167], v[110:111], v[166:167], v[214:215]\n\t"
    "v_cvt_pk_bf16_f32 v174, v164, v165\n\t"
    "s_waitcnt vmcnt(7)\n\t"
    "v_lshlrev_b32 v168, 16, v154\n\t"
    "v_and_b32 v169, 0xffff0000, v154\n\t"
    "v_pk_fma_f32 v[168:169], v[112:113], v[168:169], v[216:217]\n\t"
    "v_cvt_pk_bf16_f32 v175, v166, v167\n\t"
    "s_waitcnt vmcnt(6)\n\t"
    "v_lshlrev_b32 v170, 16, v155\n\t"
    "v_and_b32 v171, 0xffff0000, v155\n\t"
    "v_pk_fma_f32 v[170:171], v[114:115], v[170:171], v[218:219]\n\t"
    "v_cvt_pk_bf16_f32 v178, v168, v169\n\t"
    "v_cvt_pk_bf16_f32 v179, v170, v171\n\t"
    "v_lshlrev_b32 v156, 16, v172\n\t"
    "v_and_b32 v157, 0xffff0000, v172\n\t"
    "v_lshlrev_b32 v158, 16, v173\n\t"
    "v_and_b32 v159, 0xffff0000, v173\n\t"
    "v_lshlrev_b32 v160, 16, v176\n\t"
    "v_and_b32 v161, 0xffff0000, v176\n\t"
    "v_lshlrev_b32 v162, 16, v177\n\t"
    "v_and_b32 v163, 0xffff0000, v177\n\t"
    "v_lshlrev_b32 v164, 16, v174\n\t"
    "v_and_b32 v165, 0xffff0000, v174\n\t"
    "v_lshlrev_b32 v166, 16, v175\n\t"
    "v_and_b32 v167, 0xffff0000, v175\n\t"
    "v_lshlrev_b32 v168, 16, v178\n\t"
    "v_and_b32 v169, 0xffff0000, v178\n\t"
    "v_lshlrev_b32 v170, 16, v179\n\t"
    "v_and_b32 v171, 0xffff0000, v179\n\t"
    "v_pk_add_f32 v[72:73], v[156:157], v[158:159]\n\t"
    "v_pk_mul_f32 v[74:75], v[156:157], v[156:157]\n\t"
    "v_pk_fma_f32 v[74:75], v[158:159], v[158:159], v[74:75]\n\t"
    "v_pk_fma_f32 v[74:75], v[160:161], v[160:161], v[74:75]\n\t"
    "v_pk_add_f32 v[72:73], v[72:73], v[160:161]\n\t"
    "v_pk_fma_f32 v[74:75], v[162:163], v[162:163], v[74:75]\n\t"
    "v_pk_add_f32 v[72:73], v[72:73], v[162:163]\n\t"
    "v_pk_fma_f32 v[74:75], v[164:165], v[164:165], v[74:75]\n\t"
    "v_pk_add_f32 v[72:73], v[72:73], v[164:165]\n\t"
    "v_pk_fma_f32 v[74:75], v[166:167], v[166:167], v[74:75]\n\t"
    "v_pk_add_f32 v[72:73], v[72:73], v[166:167]\n\t"
    "v_pk_fma_f32 v[74:75], v[168:169], v[168:169], v[74:75]\n\t"
    "v_pk_add_f32 v[72:73], v[72:73], v[168:169]\n\t"
    "v_pk_fma_f32 v[74:75], v[170:171], v[170:171], v[74:75]\n\t"
    "v_pk_add_f32 v[72:73], v[72:73], v[170:171]\n\t"
    "v_add_f32 v72, v72, v73\n\t"
    "v_add_f32 v74, v74, v75\n\t"
    "s_nop 1\n\t"
    "v_permlane32_swap_b32 v72, v74\n\t"
    "v_add_f32 v72, v72, v74\n\t"
    "v_mov_b32 v73, v72\n\t"
    "s_nop 1\n\t"
    "v_permlane16_swap_b32 v72, v73\n\t"
    "v_add_f32 v72, v72, v73\n\t"
    "s_nop 1\n\t"
    "v_add_f32_dpp v72, v72, v72 row_ror:8 row_mask:0xf bank_mask:0xf\n\t"
    "ds_write_b32 v3, v72 offset:256\n\t"
    "buffer_store_dwordx4 v[172:175], v26, %[sw], 0 offen offset:0 sc1\n\t"
    "buffer_store_dwordx4 v[176:179], v26, %[sw], 0 offen offset:512 sc1\n\t"
    "s_waitcnt lgkmcnt(0)\n\t"
    "s_barrier\n\t"
    "ds_read_b128 v[76:79], v17 offset:256\n\t"
    "ds_read_b128 v[80:83], v17 offset:384\n\t"
    "s_or_b32 s84, s60, s64\n\t"
    "s_cmp_lg_u32 s84, 0\n\t"
    "s_cbranch_scc1 61f\n\t"
    "s_mov_b64 exec, 1\n\t"
    "buffer_store_dwordx2 v[228:229], v0, %[sw], 0 offen sc1\n\t"
    "s_mov_b64 exec, -1\n\t"
    "61:\n\t"
    "s_add_u32 m0, s82, 12288\n\t"
    "s_add_u32 s84, s66, 12288\n\t"
    "buffer_load_dwordx4 v0, %[sw], s84 offen offset:0 sc1 lds\n\t"
    "buffer_load_dwordx4 v0, %[sw], s84 offen offset:1024 sc1 lds\n\t"
    "s_waitcnt lgkmcnt(0)\n\t"
    "v_pk_add_f32 v[76:77], v[76:77], v[78:79]\n\t"
    "v_pk_add_f32 v[80:81], v[80:81], v[82:83]\n\t"
    "v_add_f32 v76, v76, v77\n\t"
    "v_add_f32 v80, v80, v81\n\t"
    "v_mul_f32 v76, 0x3b000000, v76\n\t"
    "v_mul_f32 v80, 0x3b000000, v80\n\t"
    "v_fma_f32 v80, -v76, v76, v80\n\t"
    "v_max_f32 v80, 0, v80\n\t"
    "v_add_f32 v80, 0x3727c5ac, v80\n\t"
    "v_rsq_f32 v80, v80\n\t"
    "s_nop 0\n\t"
    "v_mul_f32 v76, -v76, v80\n\t"
    "v_pk_fma_f32 v[156:157], v[156:157], v[80:81], v[76:77] op_sel_hi:[1,0,0]\n\t"
    "v_pk_fma_f32 v[158:159], v[158:159], v[80:81], v[76:77] op_sel_hi:[1,0,0]\n\t"
    "v_pk_fma_f32 v[160:161], v[160:161], v[80:81], v[76:77] op_sel_hi:[1,0,0]\n\t"
    "v_pk_fma_f32 v[162:163], v[162:163], v[80:81], v[76:77] op_sel_hi:[1,0,0]\n\t"
    "v_pk_fma_f32 v[164:165], v[164:165], v[80:81], v[76:77] op_sel_hi:[1,0,0]\n\t"
    "v_pk_fma_f32 v[166:167], v[166:167], v[80:81], v[76:77] op_sel_hi:[1,0,0]\n\t"
    "v_pk_fma_f32 v[168:169], v[168:169], v[80:81], v[76:77] op_sel_hi:[1,0,0]\n\t"
    "v_pk_fma_f32 v[170:171], v[170:171], v[80:81], v[76:77] op_sel_hi:[1,0,0]\n\t"
    "v_pk_fma_f32 v[156:157], v[156:157], v[116:117], v[132:133]\n\t"
    "v_pk_fma_f32 v[158:159], v[158:159], v[118:119], v[134:135]\n\t"
    "v_pk_fma_f32 v[164:165], v[164:165], v[124:125], v[140:141]\n\t"
    "v_pk_fma_f32 v[166:167], v[166:167], v[126:127], v[142:143]\n\t"
    "v_pk_fma_f32 v[160:161], v[160:161], v[120:121], v[136:137]\n\t"
    "v_pk_fma_f32 v[162:163], v[162:163], v[122:123], v[138:139]\n\t"
    "v_pk_fma_f32 v[168:169], v[168:169], v[128:129], v[144:145]\n\t"
    "v_pk_fma_f32 v[170:171], v[170:171], v[130:131], v[146:147]\n\t"
    "v_cvt_pk_bf16_f32 v180, v156, v157\n\t"
    "v_cvt_pk_bf16_f32 v181, v158, v159\n\t"
    "v_cvt_pk_bf16_f32 v182, v164, v165\n\t"
    "v_cvt_pk_bf16_f32 v183, v166, v167\n\t"
    "buffer_store_dwordx4 v[180:183], v27, %[sw], 0 offen offset:0 sc1\n\t"
    "v_cvt_pk_bf16_f32 v184, v160, v161\n\t"
    "v_cvt_pk_bf16_f32 v185, v162, v163\n\t"
    "v_cvt_pk_bf16_f32 v186, v168, v169\n\t"
    "v_cvt_pk_bf16_f32 v187, v170, v171\n\t"
    "buffer_store_dwordx4 v[184:187], v27, %[sw], 0 offen offset:256 sc1\n\t"
    "buffer_load_dwordx4 v0, %[sw], s84 offen offset:2048 sc1 lds\n\t"
    "s_add_u32 m0, s82, 61440\n\t"
    "s_add_u32 s84, s66, 61440\n\t"
    "buffer_load_dwordx4 v0, %[sw], s84 offen offset:0 sc1 lds\n\t"
    "buffer_load_dwordx4 v0, %[sw], s84 offen offset:1024 sc1 lds\n\t"
    "s_add_u32 m0, s83, 98304\n\t"
    "s_add_u32 s84, s76, 0\n\t"
    "buffer_load_dwordx4 v6, %[sb4], s84 offen offset:0 lds\n\t"
    "s_add_u32 s84, s76, 27648\n\t"
    "buffer_load_dwordx4 v6, %[sb4], s84 offen offset:1024 lds\n\t"
    "s_add_u32 s84, s76, 55296\n\t"
    "buffer_load_dwordx4 v6, %[sb4], s84 offen offset:2048 lds\n\t"
    "s_add_u32 s84, s76, 82944\n\t"
    "buffer_load_dwordx4 v6, %[sb4], s84 offen offset:3072 lds\n\t"
    "s_add_u32 m0, s83, 106496\n\t"
    "s_add_u32 s84, s76, 229376\n\t"
    "buffer_load_dwordx4 v6, %[sb4], s84 offen offset:0 lds\n\t"
    "s_add_u32 s84, s76, 257024\n\t"
    "buffer_load_dwordx4 v6, %[sb4], s84 offen offset:1024 lds\n\t"
    "s_add_u32 s84, s76, 284672\n\t"
    "buffer_load_dwordx4 v6, %[sb4], s84 offen offset:2048 lds\n\t"
    "s_add_u32 s84, s76, 312320\n\t"
    "buffer_load_dwordx4 v6, %[sb4], s84 offen offset:3072 lds\n\t"
    "s_waitcnt vmcnt(11)\n\t"
    "s_waitcnt lgkmcnt(0)\n\t"
    "s_barrier\n\t"
    "s_cmp_lg_u32 s60, 0\n\t"
    "s_cbranch_scc1 71f\n\t"
    "s_lshl_b32 s84, s42, 7\n\t"
    "s_add_u32 s84, s84, 47104\n\t"
    "s_mov_b64 exec, 0xff\n\t"
    "buffer_store_dwordx4 v[228:231], v0, %[sw], s84 offen sc1\n\t"
    "s_mov_b64 exec, -1\n\t"
    "71:\n\t"
    "s_add_u32 m0, s82, 61440\n\t"
    "s_add_u32 s84, s66, 61440\n\t"
    "buffer_load_dwordx4 v0, %[sw], s84 offen offset:2048 sc1 lds\n\t"
    "s_add_u32 m0, s82, 24576\n\t"
    "s_add_u32 s84, s66, 24576\n\t"
    "buffer_load_dwordx4 v0, %[sw], s84 offen offset:0 sc1 lds\n\t"
    "buffer_load_dwordx4 v0, %[sw], s84 offen offset:1024 sc1 lds\n\t"
    "buffer_load_dwordx4 v0, %[sw], s84 offen offset:2048 sc1 lds\n\t"
    "s_add_u32 m0, s82, 73728\n\t"
    "s_add_u32 s84, s66, 73728\n\t"
    "buffer_load_dwordx4 v0, %[sw], s84 offen offset:0 sc1 lds\n\t"
    "buffer_load_dwordx4 v0, %[sw], s84 offen offset:1024 sc1 lds\n\t"
    "buffer_load_dwordx4 v0, %[sw], s84 offen offset:2048 sc1 lds\n\t"
    "s_add_u32 m0, s82, 36864\n\t"
    "s_add_u32 s84, s66, 36864\n\t"
    "buffer_load_dwordx4 v0, %[sw], s84 offen offset:0 sc1 lds\n\t"
    "buffer_load_dwordx2 v[216:217], v29, %[sw], 0 offen sc1\n\t"
    "buffer_load_dwordx4 v0, %[sw], s84 offen offset:1024 sc1 lds\n\t"
    "buffer_load_dwordx4 v0, %[sw], s84 offen offset:2048 sc1 lds\n\t"
    "s_add_u32 m0, s82, 86016\n\t"
    "s_add_u32 s84, s66, 86016\n\t"
    "buffer_load_dwordx4 v0, %[sw], s84 offen offset:0 sc1 lds\n\t"
    "buffer_load_dwordx4 v0, %[sw], s84 offen offset:1024 sc1 lds\n\t"
    "buffer_load_dwordx4 v0, %[sw], s84 offen offset:2048 sc1 lds\n\t"
    "v_lshrrev_b32 v18, 2, v7\n\t"
    "v_mul_u32_u24 v18, 96, v18\n\t"
    "v_and_b32 v28, 3, v7\n\t"
    "v_lshl_add_u32 v5, v28, 3, v18\n\t"
    "v_and_b32 v18, 4, v7\n\t"
    "v_lshlrev_b32 v18, 9, v18\n\t"
    "v_lshl_add_u32 v20, v28, 7, v18\n\t"
    "s_lshl_b32 s84, s45, 2\n\t"
    "s_add_u32 s84, s84, s60\n\t"
    "s_lshl_b32 s84, s84, 14\n\t"
    "v_and_b32 v18, 15, v7\n\t"
    "v_lshl_add_u32 v18, v18, 10, s84\n\t"
    "v_and_b32 v28, 16, v7\n\t"
    "v_and_b32 v31, 32, v7\n\t"
    "s_lshl_b32 s84, s46, 6\n\t"
    "s_add_u32 s85, s84, 5242880\n\t"
    "v_lshl_add_u32 v21, v28, 5, v18\n\t"
    "v_add3_u32 v21, v21, v31, s85\n\t"
    "s_lshl_b32 s84, s84, 1\n\t"
    "v_lshl_add_u32 v22, v28, 1, v18\n\t"
    "v_lshrrev_b32 v31, 1, v31\n\t"
    "v_add3_u32 v22, v22, v31, s84\n\t"
    "s_lshl_b32 s84, s60, 8\n\t"
    "s_add_u32 s84, s84, 49152\n\t"
    "v_lshrrev_b32 v18, 1, v7\n\t"
    "v_and_b32 v18, 0x18, v18\n\t"
    "v_add_u32 v23, s84, v18\n\t"
    "s_lshl_b32 s84, s43, 2\n\t"
    "s_add_u32 s84, s84, s60\n\t"
    "s_mul_i32 s68, s84, 34816\n\t"
    "s_add_u32 s68, s68, 7340032\n\t"
    "s_mul_i32 s69, s84, 100352\n\t"
    "s_mul_i32 s85, s44, 3072\n\t"
    "s_add_u32 s69, s69, s85\n\t"
    "s_add_u32 s69, s69, 9568256\n\t"
    "s_lshl_b32 s85, s84, 4\n\t"
    "s_add_u32 s85, s85, s44\n\t"
    "s_lshl_b32 s85, s85, 10\n\t"
    "s_add_u32 s59, s85, 15990784\n\t"
    "s_lshl_b32 s84, s84, 4\n\t"
    "s_add_u32 s84, s84, s44\n\t"
    "s_lshl_b32 s84, s84, 7\n\t"
    "s_add_u32 s74, s84, 79872\n\t"
    "s_lshl_b32 s84, s45, 3\n\t"
    "s_add_u32 s84, s84, s60\n\t"
    "s_lshl_b32 s84, s84, 9\n\t"
    "s_add_u32 s75, s84, 79872\n\t"
    "s_mul_i32 s84, s45, 200704\n\t"
    "s_mul_i32 s85, s60, 12288\n\t"
    "s_add_u32 s84, s84, s85\n\t"
    "s_add_u32 s84, s84, 9571328\n\t"
    "s_xor_b32 s85, s60, 0\n\t"
    "s_mul_i32 s85, s85, 50176\n\t"
    "s_add_u32 s70, s84, s85\n\t"
    "s_xor_b32 s85, s60, 1\n\t"
    "s_mul_i32 s78, s85, 12288\n\t"
    "s_add_u32 s78, s78, 0\n\t"
    "s_mul_i32 s85, s85, 50176\n\t"
    "s_add_u32 s71, s84, s85\n\t"
    "s_xor_b32 s85, s60, 2\n\t"
    "s_mul_i32 s79, s85, 12288\n\t"
    "s_add_u32 s79, s79, 4096\n\t"
    "s_mul_i32 s85, s85, 50176\n\t"
    "s_add_u32 s72, s84, s85\n\t"
    "s_xor_b32 s85, s60, 3\n\t"
    "s_mul_i32 s80, s85, 12288\n\t"
    "s_add_u32 s80, s80, 8192\n\t"
    "s_mul_i32 s85, s85, 50176\n\t"
    "s_add_u32 s73, s84, s85\n\t"
    "s_lshl_b32 s81, s60, 13\n\t"
    "s_add_u32 s81, s81, 131072\n\t"
    "s_lshr_b32 s84, s60, 1\n\t"
    "s_lshl_b32 s85, s45, 1\n\t"
    "s_add_u32 s84, s84, s85\n\t"
    "s_lshl_b32 s84, s84, 14\n\t"
    "s_lshl_b32 s85, s60, 12\n\t"
    "s_add_u32 s84, s84, s85\n\t"
    "s_add_u32 s61, s84, 15990784\n\t"
    "s_xor_b32 s47, s61, 16384\n\t"
    "s_add_u32 s62, s48, s68\n\t"
    "s_addc_u32 s63, s49, 0\n\t"
    "s_add_u32 s90, s48, s69\n\t"
    "s_addc_u32 s91, s49, 0\n\t"
    "s_add_u32 s54, s50, s76\n\t"
    "s_addc_u32 s55, s51, 0\n\t"
    "s_add_u32 s86, s52, s65\n\t"
    "s_addc_u32 s87, s53, 0\n\t"
    "s_add_u32 s42, s48, s59\n\t"
    "s_addc_u32 s43, s49, 0\n\t"
    "s_add_u32 s66, s62, 21504\n\t"
    "s_addc_u32 s67, s63, 0\n\t"
    "s_add_u32 s62, s62, 4096\n\t"
    "s_addc_u32 s63, s63, 0\n\t"
    "s_add_u32 s94, s90, 50176\n\t"
    "s_addc_u32 s95, s91, 0\n\t"
    "s_add_u32 s54, s54, 458752\n\t"
    "s_addc_u32 s55, s55, 0\n\t"
    "s_waitcnt vmcnt(5)\n\t"
    "v_cmp_ne_u64 vcc, v[216:217], s[56:57]\n\t"
    "s_cmp_eq_u64 vcc, 0\n\t"
    "s_cbranch_scc1 81f\n\t"
    "buffer_load_dwordx2 v[216:217], v29, %[sw], 0 offen sc1\n\t"
    "s_sleep 3\n\t"
    "buffer_load_dwordx2 v[218:219], v29, %[sw], 0 offen sc1\n\t"
    "s_sleep 3\n\t"
    "s_mov_b32 s88, 0x20000\n\t"
    "80:\n\t"
    "buffer_load_dwordx2 v[220:221], v29, %[sw], 0 offen sc1\n\t"
    "s_waitcnt vmcnt(2)\n\t"
    "v_cmp_ne_u64 vcc, v[216:217], s[56:57]\n\t"
    "s_cmp_eq_u64 vcc, 0\n\t"
    "s_cbranch_scc1 81f\n\t"
    "buffer_load_dwordx2 v[216:217], v29, %[sw], 0 offen sc1\n\t"
    "s_waitcnt vmcnt(2)\n\t"
    "v_cmp_ne_u64 vcc, v[218:219], s[56:57]\n\t"
    "s_cmp_eq_u64 vcc, 0\n\t"
    "s_cbranch_scc1 81f\n\t"
    "buffer_load_dwordx2 v[218:219], v29, %[sw], 0 offen sc1\n\t"
    "s_waitcnt vmcnt(2)\n\t"
    "v_cmp_ne_u64 vcc, v[220:221], s[56:57]\n\t"
    "s_cmp_eq_u64 vcc, 0\n\t"
    "s_cbranch_scc1 81f\n\t"
    "s_sub_u32 s88, s88, 1\n\t"
    "s_cmp_lg_u32 s88, 0\n\t"
    "s_cbranch_scc1 80b\n\t"
    "81:\n\t"
    "global_load_dwordx4 v[128:131], v0, s[62:63] offset:-4096 sc1\n\t"
    "global_load_dwordx4 v[132:135], v0, s[66:67] offset:-4096 sc1\n\t"
    "global_load_dwordx4 v[136:139], v0, s[62:63] offset:-3072 sc1\n\t"
    "global_load_dwordx4 v[140:143], v0, s[66:67] offset:-3072 sc1\n\t"
    "global_load_dwordx4 v[144:147], v0, s[62:63] offset:-2048 sc1\n\t"
    "global_load_dwordx4 v[148:151], v0, s[66:67] offset:-2048 sc1\n\t"
    "global_load_dwordx4 v[152:155], v0, s[62:63] offset:-1024 sc1\n\t"
    "global_load_dwordx4 v[156:159], v0, s[66:67] offset:-1024 sc1\n\t"
    "v_add_u32 v41, 49152, v5\n\t"
    "v_mov_b64 v[32:33], s[56:57]\n\t"
    "ds_read_b64_tr_b16 v[48:49], v5 offset:0\n\t"
    "ds_read_b64_tr_b16 v[50:51], v5 offset:1536\n\t"
    "ds_read_b64_tr_b16 v[52:53], v5 offset:3072\n\t"
    "ds_read_b64_tr_b16 v[54:55], v5 offset:4608\n\t"
    "ds_read_b64_tr_b16 v[56:57], v41 offset:0\n\t"
    "ds_read_b64_tr_b16 v[58:59], v41 offset:1536\n\t"
    "ds_read_b64_tr_b16 v[60:61], v41 offset:3072\n\t"
    "ds_read_b64_tr_b16 v[62:63], v41 offset:4608\n\t"
    "ds_read_b64_tr_b16 v[64:65], v5 offset:32\n\t"
    "ds_read_b64_tr_b16 v[66:67], v5 offset:1568\n\t"
    "ds_read_b64_tr_b16 v[68:69], v5 offset:3104\n\t"
    "ds_read_b64_tr_b16 v[70:71], v5 offset:4640\n\t"
    "s_waitcnt vmcnt(4)\n\t"
    "s_waitcnt lgkmcnt(8)\n\t"
    "v_mfma_f32_16x16x32_bf16 v[80:83], v[48:51], v[128:131], 0\n\t"
    "ds_read_b64_tr_b16 v[72:73], v41 offset:32\n\t"
    "global_load_dwordx4 v[160:163], v0, s[62:63] offset:0 sc1\n\t"
    "v_mfma_f32_16x16x32_bf16 v[80:83], v[52:55], v[136:139], v[80:83]\n\t"
    "ds_read_b64_tr_b16 v[74:75], v41 offset:1568\n\t"
    "v_mfma_f32_16x16x32_bf16 v[84:87], v[48:51], v[132:135], 0\n\t"
    "ds_read_b64_tr_b16 v[76:77], v41 offset:3104\n\t"
    "v_mfma_f32_16x16x32_bf16 v[84:87], v[52:55], v[140:143], v[84:87]\n\t"
    "ds_read_b64_tr_b16 v[78:79], v41 offset:4640\n\t"
    "s_waitcnt lgkmcnt(8)\n\t"
    "v_mfma_f32_16x16x32_bf16 v[88:91], v[56:59], v[128:131], 0\n\t"
    "ds_read_b64_tr_b16 v[48:49], v5 offset:64\n\t"
    "global_load_dwordx4 v[164:167], v0, s[66:67] offset:0 sc1\n\t"
    "v_mfma_f32_16x16x32_bf16 v[88:91], v[60:63], v[136:139], v[88:91]\n\t"
    "ds_read_b64_tr_b16 v[50:51], v5 offset:1600\n\t"
    "v_mfma_f32_16x16x32_bf16 v[92:95], v[56:59], v[132:135], 0\n\t"
    "ds_read_b64_tr_b16 v[52:53], v5 offset:3136\n\t"
    "v_mfma_f32_16x16x32_bf16 v[92:95], v[60:63], v[140:143], v[92:95]\n\t"
    "ds_read_b64_tr_b16 v[54:55], v5 offset:4672\n\t"
    "s_waitcnt lgkmcnt(8)\n\t"
    "v_mfma_f32_16x16x32_bf16 v[96:99], v[64:67], v[128:131], 0\n\t"
    "ds_read_b64_tr_b16 v[56:57], v41 offset:64\n\t"
    "global_load_dwordx4 v[168:171], v0, s[62:63] offset:1024 sc1\n\t"
    "v_mfma_f32_16x16x32_bf16 v[96:99], v[68:71], v[136:139], v[96:99]\n\t"
    "ds_read_b64_tr_b16 v[58:59], v41 offset:1600\n\t"
    "v_mfma_f32_16x16x32_bf16 v[100:103], v[64:67], v[132:135], 0\n\t"
    "ds_read_b64_tr_b16 v[60:61], v41 offset:3136\n\t"
    "v_mfma_f32_16x16x32_bf16 v[100:103], v[68:71], v[140:143], v[100:103]\n\t"
    "ds_read_b64_tr_b16 v[62:63], v41 offset:4672\n\t"
    "s_waitcnt lgkmcnt(8)\n\t"
    "v_mfma_f32_16x16x32_bf16 v[104:107], v[72:75], v[128:131], 0\n\t"
    "ds_read_b64_tr_b16 v[64:65], v5 offset:6144\n\t"
    "global_load_dwordx4 v[172:175], v0, s[66:67] offset:1024 sc1\n\t"
    "v_mfma_f32_16x16x32_bf16 v[104:107], v[76:79], v[136:139], v[104:107]\n\t"
    "ds_read_b64_tr_b16 v[66:67], v5 offset:7680\n\t"
    "v_mfma_f32_16x16x32_bf16 v[108:111], v[72:75], v[132:135], 0\n\t"
    "ds_read_b64_tr_b16 v[68:69], v5 offset:9216\n\t"
    "v_mfma_f32_16x16x32_bf16 v[108:111], v[76:79], v[140:143], v[108:111]\n\t"
    "ds_read_b64_tr_b16 v[70:71], v5 offset:10752\n\t"
    "s_waitcnt lgkmcnt(8)\n\t"
    "v_mfma_f32_16x16x32_bf16 v[112:115], v[48:51], v[128:131], 0\n\t"
    "ds_read_b64_tr_b16 v[72:73], v41 offset:6144\n\t"
    "global_load_dwordx4 v[176:179], v0, s[62:63] offset:2048 sc1\n\t"
    "v_mfma_f32_16x16x32_bf16 v[112:115], v[52:55], v[136:139], v[112:115]\n\t"
    "ds_read_b64_tr_b16 v[74:75], v41 offset:7680\n\t"
    "v_mfma_f32_16x16x32_bf16 v[116:119], v[48:51], v[132:135], 0\n\t"
    "ds_read_b64_tr_b16 v[76:77], v41 offset:9216\n\t"
    "v_mfma_f32_16x16x32_bf16 v[116:119], v[52:55], v[140:143], v[116:119]\n\t"
    "ds_read_b64_tr_b16 v[78:79], v41 offset:10752\n\t"
    "s_waitcnt lgkmcnt(8)\n\t"
    "v_mfma_f32_16x16x32_bf16 v[120:123], v[56:59], v[128:131], 0\n\t"
    "ds_read_b64_tr_b16 v[48:49], v5 offset:6176\n\t"
    "global_load_dwordx4 v[180:183], v0, s[66:67] offset:2048 sc1\n\t"
    "v_mfma_f32_16x16x32_bf16 v[120:123], v[60:63], v[136:139], v[120:123]\n\t"
    "ds_read_b64_tr_b16 v[50:51], v5 offset:7712\n\t"
    "v_xor_b32 v42, 0, v1\n\t"
    "v_mfma_f32_16x16x32_bf16 v[124:127], v[56:59], v[132:135], 0\n\t"
    "ds_read_b64_tr_b16 v[52:53], v5 offset:9248\n\t"
    "v_add_u32 v42, s81, v42\n\t"
    "v_mfma_f32_16x16x32_bf16 v[124:127], v[60:63], v[140:143], v[124:127]\n\t"
    "ds_read_b64_tr_b16 v[54:55], v5 offset:10784\n\t"
    "s_waitcnt vmcnt(6)\n\t"
    "s_barrier\n\t"
    "s_waitcnt lgkmcnt(8)\n\t"
    "v_mfma_f32_16x16x32_bf16 v[80:83], v[64:67], v[144:147], v[80:83]\n\t"
    "ds_read_b64_tr_b16 v[56:57], v41 offset:6176\n\t"
    "v_xor_b32 v43, 32, v1\n\t"
    "v_mfma_f32_16x16x32_bf16 v[80:83], v[68:71], v[152:155], v[80:83]\n\t"
    "ds_read_b64_tr_b16 v[58:59], v41 offset:7712\n\t"
    "global_load_dwordx4 v[184:187], v0, s[62:63] offset:3072 sc1\n\t"
    "v_mfma_f32_16x16x32_bf16 v[84:87], v[64:67], v[148:151], v[84:87]\n\t"
    "ds_read_b64_tr_b16 v[60:61], v41 offset:9248\n\t"
    "v_add_u32 v43, s81, v43\n\t"
    "v_mfma_f32_16x16x32_bf16 v[84:87], v[68:71], v[156:159], v[84:87]\n\t"
    "ds_read_b64_tr_b16 v[62:63], v41 offset:10784\n\t"
    "s_waitcnt lgkmcnt(8)\n\t"
    "v_mfma_f32_16x16x32_bf16 v[88:91], v[72:75], v[144:147], v[88:91]\n\t"
    "ds_read_b64_tr_b16 v[64:65], v5 offset:6208\n\t"
    "v_xor_b32 v44, 64, v1\n\t"
    "v_mfma_f32_16x16x32_bf16 v[88:91], v[76:79], v[152:155], v[88:91]\n\t"
    "ds_read_b64_tr_b16 v[66:67], v5 offset:7744\n\t"
    "global_load_dwordx4 v[188:191], v0, s[66:67] offset:3072 sc1\n\t"
    "v_mfma_f32_16x16x32_bf16 v[92:95], v[72:75], v[148:151], v[92:95]\n\t"
    "ds_read_b64_tr_b16 v[68:69], v5 offset:9280\n\t"
    "v_add_u32 v44, s81, v44\n\t"
    "v_mfma_f32_16x16x32_bf16 v[92:95], v[76:79], v[156:159], v[92:95]\n\t"
    "ds_read_b64_tr_b16 v[70:71], v5 offset:10816\n\t"
    "s_waitcnt lgkmcnt(8)\n\t"
    "v_mfma_f32_16x16x32_bf16 v[96:99], v[48:51], v[144:147], v[96:99]\n\t"
    "ds_read_b64_tr_b16 v[72:73], v41 offset:6208\n\t"
    "v_xor_b32 v45, 96, v1\n\t"
    "v_mfma_f32_16x16x32_bf16 v[96:99], v[52:55], v[152:155], v[96:99]\n\t"
    "ds_read_b64_tr_b16 v[74:75], v41 offset:7744\n\t"
    "s_add_u32 s62, s62, 8192\n\t"
    "s_addc_u32 s63, s63, 0\n\t"
    "v_mfma_f32_16x16x32_bf16 v[100:103], v[48:51], v[148:151], v[100:103]\n\t"
    "ds_read_b64_tr_b16 v[76:77], v41 offset:9280\n\t"
    "global_load_dwordx4 v[192:195], v0, s[62:63] offset:-4096 sc1\n\t"
    "v_mfma_f32_16x16x32_bf16 v[100:103], v[52:55], v[156:159], v[100:103]\n\t"
    "ds_read_b64_tr_b16 v[78:79], v41 offset:10816\n\t"
    "s_waitcnt lgkmcnt(8)\n\t"
    "v_mfma_f32_16x16x32_bf16 v[104:107], v[56:59], v[144:147], v[104:107]\n\t"
    "ds_read_b64_tr_b16 v[48:49], v5 offset:12288\n\t"
    "v_add_u32 v45, s81, v45\n\t"
    "v_mfma_f32_16x16x32_bf16 v[104:107], v[60:63], v[152:155], v[104:107]\n\t"
    "ds_read_b64_tr_b16 v[50:51], v5 offset:13824\n\t"
    "s_add_u32 s66, s66, 8192\n\t"
    "s_addc_u32 s67, s67, 0\n\t"
    "v_mfma_f32_16x16x32_bf16 v[108:111], v[56:59], v[148:151], v[108:111]\n\t"
    "ds_read_b64_tr_b16 v[52:53], v5 offset:15360\n\t"
    "global_load_dwordx4 v[196:199], v0, s[66:67] offset:-4096 sc1\n\t"
    "v_mfma_f32_16x16x32_bf16 v[108:111], v[60:63], v[156:159], v[108:111]\n\t"
    "ds_read_b64_tr_b16 v[54:55], v5 offset:16896\n\t"
    "s_waitcnt lgkmcnt(8)\n\t"
    "v_mfma_f32_16x16x32_bf16 v[112:115], v[64:67], v[144:147], v[112:115]\n\t"
    "ds_read_b64_tr_b16 v[56:57], v41 offset:12288\n\t"
    "v_xor_b32 v8, 0, v1\n\t"
    "v_mfma_f32_16x16x32_bf16 v[112:115], v[68:71], v[152:155], v[112:115]\n\t"
    "ds_read_b64_tr_b16 v[58:59], v41 offset:13824\n\t"
    "v_add_u32 v9, 0x10000, v8\n\t"
    "v_mfma_f32_16x16x32_bf16 v[116:119], v[64:67], v[148:151], v[116:119]\n\t"
    "ds_read_b64_tr_b16 v[60:61], v41 offset:15360\n\t"
    "global_load_dwordx4 v[200:203], v0, s[62:63] offset:-3072 sc1\n\t"
    "v_mfma_f32_16x16x32_bf16 v[116:119], v[68:71], v[156:159], v[116:119]\n\t"
    "ds_read_b64_tr_b16 v[62:63], v41 offset:16896\n\t"
    "s_waitcnt lgkmcnt(8)\n\t"
    "v_mfma_f32_16x16x32_bf16 v[120:123], v[72:75], v[144:147], v[120:123]\n\t"
    "ds_read_b64_tr_b16 v[64:65], v5 offset:12320\n\t"
    "v_xor_b32 v11, 32, v1\n\t"
    "v_mfma_f32_16x16x32_bf16 v[120:123], v[76:79], v[152:155], v[120:123]\n\t"
    "ds_read_b64_tr_b16 v[66:67], v5 offset:13856\n\t"
    "v_add_u32 v12, 0x10000, v11\n\t"
    "v_mfma_f32_16x16x32_bf16 v[124:127], v[72:75], v[148:151], v[124:127]\n\t"
    "ds_read_b64_tr_b16 v[68:69], v5 offset:15392\n\t"
    "global_load_dwordx4 v[204:207], v0, s[66:67] offset:-3072 sc1\n\t"
    "v_mfma_f32_16x16x32_bf16 v[124:127], v[76:79], v[156:159], v[124:127]\n\t"
    "ds_read_b64_tr_b16 v[70:71], v5 offset:16928\n\t"
    "s_waitcnt vmcnt(8)\n\t"
    "s_waitcnt lgkmcnt(8)\n\t"
    "v_mfma_f32_16x16x32_bf16 v[80:83], v[48:51], v[160:163], v[80:83]\n\t"
    "ds_read_b64_tr_b16 v[72:73], v41 offset:12320\n\t"
    "v_xor_b32 v14, 64, v1\n\t"
    "v_mfma_f32_16x16x32_bf16 v[80:83], v[52:55], v[168:171], v[80:83]\n\t"
    "ds_read_b64_tr_b16 v[74:75], v41 offset:13856\n\t"
    "v_add_u32 v15, 0x10000, v14\n\t"
    "v_mfma_f32_16x16x32_bf16 v[84:87], v[48:51], v[164:167], v[84:87]\n\t"
    "ds_read_b64_tr_b16 v[76:77], v41 offset:15392\n\t"
    "global_load_dwordx4 v[208:211], v0, s[62:63] offset:-2048 sc1\n\t"
    "v_mfma_f32_16x16x32_bf16 v[84:87], v[52:55], v[172:175], v[84:87]\n\t"
    "ds_read_b64_tr_b16 v[78:79], v41 offset:16928\n\t"
    "s_waitcnt lgkmcnt(8)\n\t"
    "v_mfma_f32_16x16x32_bf16 v[88:91], v[56:59], v[160:163], v[88:91]\n\t"
    "ds_read_b64_tr_b16 v[48:49], v5 offset:12352\n\t"
    "v_xor_b32 v17, 96, v1\n\t"
    "v_mfma_f32_16x16x32_bf16 v[88:91], v[60:63], v[168:171], v[88:91]\n\t"
    "ds_read_b64_tr_b16 v[50:51], v5 offset:13888\n\t"
    "v_add_u32 v18, 0x10000, v17\n\t"
    "v_mfma_f32_16x16x32_bf16 v[92:95], v[56:59], v[164:167], v[92:95]\n\t"
    "ds_read_b64_tr_b16 v[52:53], v5 offset:15424\n\t"
    "global_load_dwordx4 v[212:215], v0, s[66:67] offset:-2048 sc1\n\t"
    "v_mfma_f32_16x16x32_bf16 v[92:95], v[60:63], v[172:175], v[92:95]\n\t"
    "ds_read_b64_tr_b16 v[54:55], v5 offset:16960\n\t"
    "s_waitcnt lgkmcnt(8)\n\t"
    "v_mfma_f32_16x16x32_bf16 v[96:99], v[64:67], v[160:163], v[96:99]\n\t"
    "ds_read_b64_tr_b16 v[56:57], v41 offset:12352\n\t"
    "v_add_u32 v10, 27648, v6\n\t"
    "v_mfma_f32_16x16x32_bf16 v[96:99], v[68:71], v[168:171], v[96:99]\n\t"
    "ds_read_b64_tr_b16 v[58:59], v41 offset:13888\n\t"
    "v_add_u32 v13, 55296, v6\n\t"
    "v_mfma_f32_16x16x32_bf16 v[100:103], v[64:67], v[164:167], v[100:103]\n\t"
    "ds_read_b64_tr_b16 v[60:61], v41 offset:15424\n\t"
    "global_load_dwordx4 v[216:219], v0, s[62:63] offset:-1024 sc1\n\t"
    "v_mfma_f32_16x16x32_bf16 v[100:103], v[68:71], v[172:175], v[100:103]\n\t"
    "ds_read_b64_tr_b16 v[62:63], v41 offset:16960\n\t"
    "s_waitcnt lgkmcnt(8)\n\t"
    "v_mfma_f32_16x16x32_bf16 v[104:107], v[72:75], v[160:163], v[104:107]\n\t"
    "ds_read_b64_tr_b16 v[64:65], v5 offset:18432\n\t"
    "v_add_u32 v16, 82944, v6\n\t"
    "v_mfma_f32_16x16x32_bf16 v[104:107], v[76:79], v[168:171], v[104:107]\n\t"
    "ds_read_b64_tr_b16 v[66:67], v5 offset:19968\n\t"
    "v_add_u32 v19, 7168, v2\n\t"
    "v_mfma_f32_16x16x32_bf16 v[108:111], v[72:75], v[164:167], v[108:111]\n\t"
    "ds_read_b64_tr_b16 v[68:69], v5 offset:21504\n\t"
    "v_mfma_f32_16x16x32_bf16 v[108:111], v[76:79], v[172:175], v[108:111]\n\t"
    "ds_read_b64_tr_b16 v[70:71], v5 offset:23040\n\t"
    "s_waitcnt lgkmcnt(8)\n\t"
    "v_mfma_f32_16x16x32_bf16 v[112:115], v[48:51], v[160:163], v[112:115]\n\t"
    "ds_read_b64_tr_b16 v[72:73], v41 offset:18432\n\t"
    "global_load_dwordx4 v[220:223], v0, s[66:67] offset:-1024 sc1\n\t"
    "v_mfma_f32_16x16x32_bf16 v[112:115], v[52:55], v[168:171], v[112:115]\n\t"
    "ds_read_b64_tr_b16 v[74:75], v41 offset:19968\n\t"
    "v_mfma_f32_16x16x32_bf16 v[116:119], v[48:51], v[164:167], v[116:119]\n\t"
    "ds_read_b64_tr_b16 v[76:77], v41 offset:21504\n\t"
    "v_mfma_f32_16x16x32_bf16 v[116:119], v[52:55], v[172:175], v[116:119]\n\t"
    "ds_read_b64_tr_b16 v[78:79], v41 offset:23040\n\t"
    "s_waitcnt lgkmcnt(8)\n\t"
    "v_mfma_f32_16x16x32_bf16 v[120:123], v[56:59], v[160:163], v[120:123]\n\t"
    "ds_read_b64_tr_b16 v[48:49], v5 offset:18464\n\t"
    "global_load_dwordx4 a[224:227], v0, s[62:63] offset:0 sc1\n\t"
    "v_mfma_f32_16x16x32_bf16 v[120:123], v[60:63], v[168:171], v[120:123]\n\t"
    "ds_read_b64_tr_b16 v[50:51], v5 offset:20000\n\t"
    "v_mfma_f32_16x16x32_bf16 v[124:127], v[56:59], v[164:167], v[124:127]\n\t"
    "ds_read_b64_tr_b16 v[52:53], v5 offset:21536\n\t"
    "v_mfma_f32_16x16x32_bf16 v[124:127], v[60:63], v[172:175], v[124:127]\n\t"
    "ds_read_b64_tr_b16 v[54:55], v5 offset:23072\n\t"
    "global_load_dwordx4 a[228:231], v0, s[66:67] offset:0 sc1\n\t"
    "s_waitcnt vmcnt(10)\n\t"
    "s_waitcnt lgkmcnt(8)\n\t"
    "v_mfma_f32_16x16x32_bf16 v[80:83], v[64:67], v[176:179], v[80:83]\n\t"
    "ds_read_b64_tr_b16 v[56:57], v41 offset:18464\n\t"
    "v_mfma_f32_16x16x32_bf16 v[80:83], v[68:71], v[184:187], v[80:83]\n\t"
    "ds_read_b64_tr_b16 v[58:59], v41 offset:20000\n\t"
    "v_mfma_f32_16x16x32_bf16 v[84:87], v[64:67], v[180:183], v[84:87]\n\t"
    "ds_read_b64_tr_b16 v[60:61], v41 offset:21536\n\t"
    "v_mfma_f32_16x16x32_bf16 v[84:87], v[68:71], v[188:191], v[84:87]\n\t"
    "ds_read_b64_tr_b16 v[62:63], v41 offset:23072\n\t"
    "s_waitcnt lgkmcnt(8)\n\t"
    "v_mfma_f32_16x16x32_bf16 v[88:91], v[72:75], v[176:179], v[88:91]\n\t"
    "ds_read_b64_tr_b16 v[64:65], v5 offset:18496\n\t"
    "global_load_dwordx4 a[232:235], v0, s[62:63] offset:1024 sc1\n\t"
    "v_mfma_f32_16x16x32_bf16 v[88:91], v[76:79], v[184:187], v[88:91]\n\t"
    "ds_read_b64_tr_b16 v[66:67], v5 offset:20032\n\t"
    "v_mfma_f32_16x16x32_bf16 v[92:95], v[72:75], v[180:183], v[92:95]\n\t"
    "ds_read_b64_tr_b16 v[68:69], v5 offset:21568\n\t"
    "v_mfma_f32_16x16x32_bf16 v[92:95], v[76:79], v[188:191], v[92:95]\n\t"
    "ds_read_b64_tr_b16 v[70:71], v5 offset:23104\n\t"
    "s_waitcnt lgkmcnt(8)\n\t"
    "v_mfma_f32_16x16x32_bf16 v[96:99], v[48:51], v[176:179], v[96:99]\n\t"
    "ds_read_b64_tr_b16 v[72:73], v41 offset:18496\n\t"
    "global_load_dwordx4 a[236:239], v0, s[66:67] offset:1024 sc1\n\t"
    "v_mfma_f32_16x16x32_bf16 v[96:99], v[52:55], v[184:187], v[96:99]\n\t"
    "ds_read_b64_tr_b16 v[74:75], v41 offset:20032\n\t"
    "v_mfma_f32_16x16x32_bf16 v[100:103], v[48:51], v[180:183], v[100:103]\n\t"
    "ds_read_b64_tr_b16 v[76:77], v41 offset:21568\n\t"
    "v_mfma_f32_16x16x32_bf16 v[100:103], v[52:55], v[188:191], v[100:103]\n\t"
    "ds_read_b64_tr_b16 v[78:79], v41 offset:23104\n\t"
    "s_waitcnt lgkmcnt(8)\n\t"
    "v_mfma_f32_16x16x32_bf16 v[104:107], v[56:59], v[176:179], v[104:107]\n\t"
    "ds_read_b64_tr_b16 v[48:49], v5 offset:24576\n\t"
    "global_load_dwordx4 a[240:243], v0, s[62:63] offset:2048 sc1\n\t"
    "v_mfma_f32_16x16x32_bf16 v[104:107], v[60:63], v[184:187], v[104:107]\n\t"
    "ds_read_b64_tr_b16 v[50:51], v5 offset:26112\n\t"
    "v_mfma_f32_16x16x32_bf16 v[108:111], v[56:59], v[180:183], v[108:111]\n\t"
    "ds_read_b64_tr_b16 v[52:53], v5 offset:27648\n\t"
    "v_mfma_f32_16x16x32_bf16 v[108:111], v[60:63], v[188:191], v[108:111]\n\t"
    "ds_read_b64_tr_b16 v[54:55], v5 offset:29184\n\t"
    "s_waitcnt lgkmcnt(8)\n\t"
    "v_mfma_f32_16x16x32_bf16 v[112:115], v[64:67], v[176:179], v[112:115]\n\t"
    "ds_read_b64_tr_b16 v[56:57], v41 offset:24576\n\t"
    "global_load_dwordx4 a[244:247], v0, s[66:67] offset:2048 sc1\n\t"
    "v_mfma_f32_16x16x32_bf16 v[112:115], v[68:71], v[184:187], v[112:115]\n\t"
    "ds_read_b64_tr_b16 v[58:59], v41 offset:26112\n\t"
    "v_mfma_f32_16x16x32_bf16 v[116:119], v[64:67], v[180:183], v[116:119]\n\t"
    "ds_read_b64_tr_b16 v[60:61], v41 offset:27648\n\t"
    "v_mfma_f32_16x16x32_bf16 v[116:119], v[68:71], v[188:191], v[116:119]\n\t"
    "ds_read_b64_tr_b16 v[62:63], v41 offset:29184\n\t"
    "s_waitcnt lgkmcnt(8)\n\t"
    "v_mfma_f32_16x16x32_bf16 v[120:123], v[72:75], v[176:179], v[120:123]\n\t"
    "ds_read_b64_tr_b16 v[64:65], v5 offset:24608\n\t"
    "global_load_dwordx4 a[248:251], v0, s[62:63] offset:3072 sc1\n\t"
    "v_mfma_f32_16x16x32_bf16 v[120:123], v[76:79], v[184:187], v[120:123]\n\t"
    "ds_read_b64_tr_b16 v[66:67], v5 offset:26144\n\t"
    "v_mfma_f32_16x16x32_bf16 v[124:127], v[72:75], v[180:183], v[124:127]\n\t"
    "ds_read_b64_tr_b16 v[68:69], v5 offset:27680\n\t"
    "v_mfma_f32_16x16x32_bf16 v[124:127], v[76:79], v[188:191], v[124:127]\n\t"
    "ds_read_b64_tr_b16 v[70:71], v5 offset:29216\n\t"
    "s_barrier\n\t"
    "s_waitcnt vmcnt(11)\n\t"
    "s_waitcnt lgkmcnt(8)\n\t"
    "v_mfma_f32_16x16x32_bf16 v[80:83], v[48:51], v[192:195], v[80:83]\n\t"
    "ds_read_b64_tr_b16 v[72:73], v41 offset:24608\n\t"
    "global_load_dwordx4 a[252:255], v0, s[66:67] offset:3072 sc1\n\t"
    "v_mfma_f32_16x16x32_bf16 v[80:83], v[52:55], v[200:203], v[80:83]\n\t"
    "ds_read_b64_tr_b16 v[74:75], v41 offset:26144\n\t"
    "v_mfma_f32_16x16x32_bf16 v[84:87], v[48:51], v[196:199], v[84:87]\n\t"
    "ds_read_b64_tr_b16 v[76:77], v41 offset:27680\n\t"
    "v_mfma_f32_16x16x32_bf16 v[84:87], v[52:55], v[204:207], v[84:87]\n\t"
    "ds_read_b64_tr_b16 v[78:79], v41 offset:29216\n\t"
    "s_waitcnt lgkmcnt(8)\n\t"
    "v_mfma_f32_16x16x32_bf16 v[88:91], v[56:59], v[192:195], v[88:91]\n\t"
    "ds_read_b64_tr_b16 v[48:49], v5 offset:24640\n\t"
    "s_add_u32 m0, s77, 0\n\t"
    "v_mfma_f32_16x16x32_bf16 v[88:91], v[60:63], v[200:203], v[88:91]\n\t"
    "ds_read_b64_tr_b16 v[50:51], v5 offset:26176\n\t"
    "global_load_lds_dwordx4 v6, s[54:55] offset:0\n\t"
    "v_mfma_f32_16x16x32_bf16 v[92:95], v[56:59], v[196:199], v[92:95]\n\t"
    "ds_read_b64_tr_b16 v[52:53], v5 offset:27712\n\t"
    "v_mfma_f32_16x16x32_bf16 v[92:95], v[60:63], v[204:207], v[92:95]\n\t"
    "ds_read_b64_tr_b16 v[54:55], v5 offset:29248\n\t"
    "s_waitcnt lgkmcnt(8)\n\t"
    "v_mfma_f32_16x16x32_bf16 v[96:99], v[64:67], v[192:195], v[96:99]\n\t"
    "ds_read_b64_tr_b16 v[56:57], v41 offset:24640\n\t"
    "v_mfma_f32_16x16x32_bf16 v[96:99], v[68:71], v[200:203], v[96:99]\n\t"
    "ds_read_b64_tr_b16 v[58:59], v41 offset:26176\n\t"
    "v_mfma_f32_16x16x32_bf16 v[100:103], v[64:67], v[196:199], v[100:103]\n\t"
    "ds_read_b64_tr_b16 v[60:61], v41 offset:27712\n\t"
    "global_load_lds_dwordx4 v10, s[54:55] offset:1024\n\t"
    "v_mfma_f32_16x16x32_bf16 v[100:103], v[68:71], v[204:207], v[100:103]\n\t"
    "ds_read_b64_tr_b16 v[62:63], v41 offset:29248\n\t"
    "s_waitcnt lgkmcnt(8)\n\t"
    "v_mfma_f32_16x16x32_bf16 v[104:107], v[72:75], v[192:195], v[104:107]\n\t"
    "ds_read_b64_tr_b16 v[64:65], v5 offset:30720\n\t"
    "v_mfma_f32_16x16x32_bf16 v[104:107], v[76:79], v[200:203], v[104:107]\n\t"
    "ds_read_b64_tr_b16 v[66:67], v5 offset:32256\n\t"
    "v_mfma_f32_16x16x32_bf16 v[108:111], v[72:75], v[196:199], v[108:111]\n\t"
    "ds_read_b64_tr_b16 v[68:69], v5 offset:33792\n\t"
    "global_load_lds_dwordx4 v13, s[54:55] offset:2048\n\t"
    "v_mfma_f32_16x16x32_bf16 v[108:111], v[76:79], v[204:207], v[108:111]\n\t"
    "ds_read_b64_tr_b16 v[70:71], v5 offset:35328\n\t"
    "s_waitcnt lgkmcnt(8)\n\t"
    "v_mfma_f32_16x16x32_bf16 v[112:115], v[48:51], v[192:195], v[112:115]\n\t"
    "ds_read_b64_tr_b16 v[72:73], v41 offset:30720\n\t"
    "v_mfma_f32_16x16x32_bf16 v[112:115], v[52:55], v[200:203], v[112:115]\n\t"
    "ds_read_b64_tr_b16 v[74:75], v41 offset:32256\n\t"
    "v_mfma_f32_16x16x32_bf16 v[116:119], v[48:51], v[196:199], v[116:119]\n\t"
    "ds_read_b64_tr_b16 v[76:77], v41 offset:33792\n\t"
    "global_load_lds_dwordx4 v16, s[54:55] offset:3072\n\t"
    "v_mfma_f32_16x16x32_bf16 v[116:119], v[52:55], v[204:207], v[116:119]\n\t"
    "ds_read_b64_tr_b16 v[78:79], v41 offset:35328\n\t"
    "s_waitcnt lgkmcnt(8)\n\t"
    "v_mfma_f32_16x16x32_bf16 v[120:123], v[56:59], v[192:195], v[120:123]\n\t"
    "ds_read_b64_tr_b16 v[48:49], v5 offset:30752\n\t"
    "v_mfma_f32_16x16x32_bf16 v[120:123], v[60:63], v[200:203], v[120:123]\n\t"
    "ds_read_b64_tr_b16 v[50:51], v5 offset:32288\n\t"
    "s_add_u32 s54, s54, 229376\n\t"
    "s_addc_u32 s55, s55, 0\n\t"
    "v_mfma_f32_16x16x32_bf16 v[124:127], v[56:59], v[196:199], v[124:127]\n\t"
    "ds_read_b64_tr_b16 v[52:53], v5 offset:33824\n\t"
    "s_add_u32 m0, s77, 8192\n\t"
    "v_mfma_f32_16x16x32_bf16 v[124:127], v[60:63], v[204:207], v[124:127]\n\t"
    "ds_read_b64_tr_b16 v[54:55], v5 offset:35360\n\t"
    "global_load_lds_dwordx4 v6, s[54:55] offset:0\n\t"
    "s_waitcnt vmcnt(13)\n\t"
    "s_waitcnt lgkmcnt(8)\n\t"
    "v_mfma_f32_16x16x32_bf16 v[80:83], v[64:67], v[208:211], v[80:83]\n\t"
    "ds_read_b64_tr_b16 v[56:57], v41 offset:30752\n\t"
    "v_mfma_f32_16x16x32_bf16 v[80:83], v[68:71], v[216:219], v[80:83]\n\t"
    "ds_read_b64_tr_b16 v[58:59], v41 offset:32288\n\t"
    "v_mfma_f32_16x16x32_bf16 v[84:87], v[64:67], v[212:215], v[84:87]\n\t"
    "ds_read_b64_tr_b16 v[60:61], v41 offset:33824\n\t"
    "global_load_lds_dwordx4 v10, s[54:55] offset:1024\n\t"
    "v_mfma_f32_16x16x32_bf16 v[84:87], v[68:71], v[220:223], v[84:87]\n\t"
    "ds_read_b64_tr_b16 v[62:63], v41 offset:35360\n\t"
    "s_waitcnt lgkmcnt(8)\n\t"
    "v_mfma_f32_16x16x32_bf16 v[88:91], v[72:75], v[208:211], v[88:91]\n\t"
    "ds_read_b64_tr_b16 v[64:65], v5 offset:30784\n\t"
    "v_mfma_f32_16x16x32_bf16 v[88:91], v[76:79], v[216:219], v[88:91]\n\t"
    "ds_read_b64_tr_b16 v[66:67], v5 offset:32320\n\t"
    "v_mfma_f32_16x16x32_bf16 v[92:95], v[72:75], v[212:215], v[92:95]\n\t"
    "ds_read_b64_tr_b16 v[68:69], v5 offset:33856\n\t"
    "v_mfma_f32_16x16x32_bf16 v[92:95], v[76:79], v[220:223], v[92:95]\n\t"
    "ds_read_b64_tr_b16 v[70:71], v5 offset:35392\n\t"
    "s_waitcnt lgkmcnt(8)\n\t"
    "v_mfma_f32_16x16x32_bf16 v[96:99], v[48:51], v[208:211], v[96:99]\n\t"
    "ds_read_b64_tr_b16 v[72:73], v41 offset:30784\n\t"
    "global_load_lds_dwordx4 v13, s[54:55] offset:2048\n\t"
    "v_mfma_f32_16x16x32_bf16 v[96:99], v[52:55], v[216:219], v[96:99]\n\t"
    "ds_read_b64_tr_b16 v[74:75], v41 offset:32320\n\t"
    "v_mfma_f32_16x16x32_bf16 v[100:103], v[48:51], v[212:215], v[100:103]\n\t"
    "ds_read_b64_tr_b16 v[76:77], v41 offset:33856\n\t"
    "v_mfma_f32_16x16x32_bf16 v[100:103], v[52:55], v[220:223], v[100:103]\n\t"
    "ds_read_b64_tr_b16 v[78:79], v41 offset:35392\n\t"
    "s_waitcnt lgkmcnt(8)\n\t"
    "v_mfma_f32_16x16x32_bf16 v[104:107], v[56:59], v[208:211], v[104:107]\n\t"
    "ds_read_b64_tr_b16 v[48:49], v5 offset:36864\n\t"
    "global_load_lds_dwordx4 v16, s[54:55] offset:3072\n\t"
    "v_mfma_f32_16x16x32_bf16 v[104:107], v[60:63], v[216:219], v[104:107]\n\t"
    "ds_read_b64_tr_b16 v[50:51], v5 offset:38400\n\t"
    "v_mfma_f32_16x16x32_bf16 v[108:111], v[56:59], v[212:215], v[108:111]\n\t"
    "ds_read_b64_tr_b16 v[52:53], v5 offset:39936\n\t"
    "s_add_u32 s54, s54, 229376\n\t"
    "s_addc_u32 s55, s55, 0\n\t"
    "v_mfma_f32_16x16x32_bf16 v[108:111], v[60:63], v[220:223], v[108:111]\n\t"
    "ds_read_b64_tr_b16 v[54:55], v5 offset:41472\n\t"
    "s_waitcnt lgkmcnt(8)\n\t"
    "v_mfma_f32_16x16x32_bf16 v[112:115], v[64:67], v[208:211], v[112:115]\n\t"
    "ds_read_b64_tr_b16 v[56:57], v41 offset:36864\n\t"
    "s_add_u32 m0, s77, 16384\n\t"
    "v_mfma_f32_16x16x32_bf16 v[112:115], v[68:71], v[216:219], v[112:115]\n\t"
    "ds_read_b64_tr_b16 v[58:59], v41 offset:38400\n\t"
    "global_load_lds_dwordx4 v6, s[54:55] offset:0\n\t"
    "v_mfma_f32_16x16x32_bf16 v[116:119], v[64:67], v[212:215], v[116:119]\n\t"
    "ds_read_b64_tr_b16 v[60:61], v41 offset:39936\n\t"
    "v_mfma_f32_16x16x32_bf16 v[116:119], v[68:71], v[220:223], v[116:119]\n\t"
    "ds_read_b64_tr_b16 v[62:63], v41 offset:41472\n\t"
    "s_waitcnt lgkmcnt(8)\n\t"
    "v_mfma_f32_16x16x32_bf16 v[120:123], v[72:75], v[208:211], v[120:123]\n\t"
    "ds_read_b64_tr_b16 v[64:65], v5 offset:36896\n\t"
    "global_load_lds_dwordx4 v10, s[54:55] offset:1024\n\t"
    "v_mfma_f32_16x16x32_bf16 v[120:123], v[76:79], v[216:219], v[120:123]\n\t"
    "ds_read_b64_tr_b16 v[66:67], v5 offset:38432\n\t"
    "v_mfma_f32_16x16x32_bf16 v[124:127], v[72:75], v[212:215], v[124:127]\n\t"
    "ds_read_b64_tr_b16 v[68:69], v5 offset:39968\n\t"
    "v_mfma_f32_16x16x32_bf16 v[124:127], v[76:79], v[220:223], v[124:127]\n\t"
    "ds_read_b64_tr_b16 v[70:71], v5 offset:41504\n\t"
    "s_barrier\n\t"
    "global_load_lds_dwordx4 v13, s[54:55] offset:2048\n\t"
    "s_waitcnt vmcnt(15)\n\t"
    "s_waitcnt lgkmcnt(8)\n\t"
    "v_mfma_f32_16x16x32_bf16 v[80:83], v[48:51], a[224:227], v[80:83]\n\t"
    "ds_read_b64_tr_b16 v[72:73], v41 offset:36896\n\t"
    "v_mfma_f32_16x16x32_bf16 v[80:83], v[52:55], a[232:235], v[80:83]\n\t"
    "ds_read_b64_tr_b16 v[74:75], v41 offset:38432\n\t"
    "v_mfma_f32_16x16x32_bf16 v[84:87], v[48:51], a[228:231], v[84:87]\n\t"
    "ds_read_b64_tr_b16 v[76:77], v41 offset:39968\n\t"
    "v_mfma_f32_16x16x32_bf16 v[84:87], v[52:55], a[236:239], v[84:87]\n\t"
    "ds_read_b64_tr_b16 v[78:79], v41 offset:41504\n\t"
    "s_waitcnt lgkmcnt(8)\n\t"
    "v_mfma_f32_16x16x32_bf16 v[88:91], v[56:59], a[224:227], v[88:91]\n\t"
    "ds_read_b64_tr_b16 v[48:49], v5 offset:36928\n\t"
    "global_load_lds_dwordx4 v16, s[54:55] offset:3072\n\t"
    "v_mfma_f32_16x16x32_bf16 v[88:91], v[60:63], a[232:235], v[88:91]\n\t"
    "ds_read_b64_tr_b16 v[50:51], v5 offset:38464\n\t"
    "v_mfma_f32_16x16x32_bf16 v[92:95], v[56:59], a[228:231], v[92:95]\n\t"
    "ds_read_b64_tr_b16 v[52:53], v5 offset:40000\n\t"
    "v_mfma_f32_16x16x32_bf16 v[92:95], v[60:63], a[236:239], v[92:95]\n\t"
    "ds_read_b64_tr_b16 v[54:55], v5 offset:41536\n\t"
    "s_waitcnt lgkmcnt(8)\n\t"
    "v_mfma_f32_16x16x32_bf16 v[96:99], v[64:67], a[224:227], v[96:99]\n\t"
    "ds_read_b64_tr_b16 v[56:57], v41 offset:36928\n\t"
    "s_add_u32 s54, s54, 229376\n\t"
    "s_addc_u32 s55, s55, 0\n\t"
    "v_mfma_f32_16x16x32_bf16 v[96:99], v[68:71], a[232:235], v[96:99]\n\t"
    "ds_read_b64_tr_b16 v[58:59], v41 offset:38464\n\t"
    "s_add_u32 m0, s77, 24576\n\t"
    "v_mfma_f32_16x16x32_bf16 v[100:103], v[64:67], a[228:231], v[100:103]\n\t"
    "ds_read_b64_tr_b16 v[60:61], v41 offset:40000\n\t"
    "global_load_lds_dwordx4 v6, s[54:55] offset:0\n\t"
    "v_mfma_f32_16x16x32_bf16 v[100:103], v[68:71], a[236:239], v[100:103]\n\t"
    "ds_read_b64_tr_b16 v[62:63], v41 offset:41536\n\t"
    "s_waitcnt lgkmcnt(8)\n\t"
    "v_mfma_f32_16x16x32_bf16 v[104:107], v[72:75], a[224:227], v[104:107]\n\t"
    "ds_read_b64_tr_b16 v[64:65], v5 offset:43008\n\t"
    "v_mfma_f32_16x16x32_bf16 v[104:107], v[76:79], a[232:235], v[104:107]\n\t"
    "ds_read_b64_tr_b16 v[66:67], v5 offset:44544\n\t"
    "global_load_lds_dwordx4 v10, s[54:55] offset:1024\n\t"
    "v_mfma_f32_16x16x32_bf16 v[108:111], v[72:75], a[228:231], v[108:111]\n\t"
    "ds_read_b64_tr_b16 v[68:69], v5 offset:46080\n\t"
    "v_mfma_f32_16x16x32_bf16 v[108:111], v[76:79], a[236:239], v[108:111]\n\t"
    "ds_read_b64_tr_b16 v[70:71], v5 offset:47616\n\t"
    "s_waitcnt lgkmcnt(8)\n\t"
    "v_mfma_f32_16x16x32_bf16 v[112:115], v[48:51], a[224:227], v[112:115]\n\t"
    "ds_read_b64_tr_b16 v[72:73], v41 offset:43008\n\t"
    "v_mfma_f32_16x16x32_bf16 v[112:115], v[52:55], a[232:235], v[112:115]\n\t"
    "ds_read_b64_tr_b16 v[74:75], v41 offset:44544\n\t"
    "global_load_lds_dwordx4 v13, s[54:55] offset:2048\n\t"
    "v_mfma_f32_16x16x32_bf16 v[116:119], v[48:51], a[228:231], v[116:119]\n\t"
    "ds_read_b64_tr_b16 v[76:77], v41 offset:46080\n\t"
    "v_mfma_f32_16x16x32_bf16 v[116:119], v[52:55], a[236:239], v[116:119]\n\t"
    "ds_read_b64_tr_b16 v[78:79], v41 offset:47616\n\t"
    "s_waitcnt lgkmcnt(8)\n\t"
    "v_mfma_f32_16x16x32_bf16 v[120:123], v[56:59], a[224:227], v[120:123]\n\t"
    "ds_read_b64_tr_b16 v[48:49], v5 offset:43040\n\t"
    "v_mfma_f32_16x16x32_bf16 v[120:123], v[60:63], a[232:235], v[120:123]\n\t"
    "ds_read_b64_tr_b16 v[50:51], v5 offset:44576\n\t"
    "global_load_lds_dwordx4 v16, s[54:55] offset:3072\n\t"
    "v_mfma_f32_16x16x32_bf16 v[124:127], v[56:59], a[228:231], v[124:127]\n\t"
    "ds_read_b64_tr_b16 v[52:53], v5 offset:46112\n\t"
    "v_mfma_f32_16x16x32_bf16 v[124:127], v[60:63], a[236:239], v[124:127]\n\t"
    "ds_read_b64_tr_b16 v[54:55], v5 offset:47648\n\t"
    "s_waitcnt vmcnt(16)\n\t"
    "s_waitcnt lgkmcnt(8)\n\t"
    "v_mfma_f32_16x16x32_bf16 v[80:83], v[64:67], a[240:243], v[80:83]\n\t"
    "ds_read_b64_tr_b16 v[56:57], v41 offset:43040\n\t"
    "v_mfma_f32_16x16x32_bf16 v[80:83], v[68:71], a[248:251], v[80:83]\n\t"
    "ds_read_b64_tr_b16 v[58:59], v41 offset:44576\n\t"
    "v_mfma_f32_16x16x32_bf16 v[84:87], v[64:67], a[244:247], v[84:87]\n\t"
    "ds_read_b64_tr_b16 v[60:61], v41 offset:46112\n\t"
    "s_add_u32 s68, s86, 262144\n\t"
    "s_addc_u32 s69, s87, 0\n\t"
    "v_mfma_f32_16x16x32_bf16 v[84:87], v[68:71], a[252:255], v[84:87]\n\t"
    "ds_read_b64_tr_b16 v[62:63], v41 offset:47648\n\t"
    "s_waitcnt lgkmcnt(8)\n\t"
    "v_mfma_f32_16x16x32_bf16 v[88:91], v[72:75], a[240:243], v[88:91]\n\t"
    "ds_read_b64_tr_b16 v[64:65], v5 offset:43072\n\t"
    "s_add_u32 m0, s81, 0\n\t"
    "v_mfma_f32_16x16x32_bf16 v[88:91], v[76:79], a[248:251], v[88:91]\n\t"
    "ds_read_b64_tr_b16 v[66:67], v5 offset:44608\n\t"
    "global_load_lds_dwordx4 v2, s[68:69] offset:0\n\t"
    "v_mfma_f32_16x16x32_bf16 v[92:95], v[72:75], a[244:247], v[92:95]\n\t"
    "ds_read_b64_tr_b16 v[68:69], v5 offset:46144\n\t"
    "v_mfma_f32_16x16x32_bf16 v[92:95], v[76:79], a[252:255], v[92:95]\n\t"
    "ds_read_b64_tr_b16 v[70:71], v5 offset:47680\n\t"
    "s_waitcnt lgkmcnt(8)\n\t"
    "v_mfma_f32_16x16x32_bf16 v[96:99], v[48:51], a[240:243], v[96:99]\n\t"
    "ds_read_b64_tr_b16 v[72:73], v41 offset:43072\n\t"
    "global_load_lds_dwordx4 v19, s[68:69] offset:1024\n\t"
    "v_mfma_f32_16x16x32_bf16 v[96:99], v[52:55], a[248:251], v[96:99]\n\t"
    "ds_read_b64_tr_b16 v[74:75], v41 offset:44608\n\t"
    "v_mfma_f32_16x16x32_bf16 v[100:103], v[48:51], a[244:247], v[100:103]\n\t"
    "ds_read_b64_tr_b16 v[76:77], v41 offset:46144\n\t"
    "s_add_u32 s68, s86, 276480\n\t"
    "s_addc_u32 s69, s87, 0\n\t"
    "v_mfma_f32_16x16x32_bf16 v[100:103], v[52:55], a[252:255], v[100:103]\n\t"
    "ds_read_b64_tr_b16 v[78:79], v41 offset:47680\n\t"
    "s_waitcnt lgkmcnt(8)\n\t"
    "v_mfma_f32_16x16x32_bf16 v[104:107], v[56:59], a[240:243], v[104:107]\n\t"
    "ds_read_b64_tr_b16 v[48:49], v9 offset:32768\n\t"
    "v_mfma_f32_16x16x32_bf16 v[104:107], v[60:63], a[248:251], v[104:107]\n\t"
    "ds_read_b64_tr_b16 v[50:51], v9 offset:34816\n\t"
    "global_load_lds_dwordx4 v2, s[68:69] offset:2048\n\t"
    "v_mfma_f32_16x16x32_bf16 v[108:111], v[56:59], a[244:247], v[108:111]\n\t"
    "ds_read_b64_tr_b16 v[52:53], v9 offset:36864\n\t"
    "v_mfma_f32_16x16x32_bf16 v[108:111], v[60:63], a[252:255], v[108:111]\n\t"
    "ds_read_b64_tr_b16 v[54:55], v9 offset:38912\n\t"
    "s_waitcnt lgkmcnt(8)\n\t"
    "v_mfma_f32_16x16x32_bf16 v[112:115], v[64:67], a[240:243], v[112:115]\n\t"
    "ds_read_b64_tr_b16 v[56:57], v9 offset:49152\n\t"
    "global_load_lds_dwordx4 v19, s[68:69] offset:3072\n\t"
    "v_mfma_f32_16x16x32_bf16 v[112:115], v[68:71], a[248:251], v[112:115]\n\t"
    "ds_read_b64_tr_b16 v[58:59], v9 offset:51200\n\t"
    "v_mfma_f32_16x16x32_bf16 v[116:119], v[64:67], a[244:247], v[116:119]\n\t"
    "ds_read_b64_tr_b16 v[60:61], v9 offset:53248\n\t"
    "v_mfma_f32_16x16x32_bf16 v[116:119], v[68:71], a[252:255], v[116:119]\n\t"
    "ds_read_b64_tr_b16 v[62:63], v9 offset:55296\n\t"
    "s_waitcnt lgkmcnt(8)\n\t"
    "v_mfma_f32_16x16x32_bf16 v[120:123], v[72:75], a[240:243], v[120:123]\n\t"
    "ds_read_b64_tr_b16 v[64:65], v12 offset:32768\n\t"
    "s_add_u32 s68, s86, 393216\n\t"
    "s_addc_u32 s69, s87, 0\n\t"
    "v_mfma_f32_16x16x32_bf16 v[120:123], v[76:79], a[248:251], v[120:123]\n\t"
    "ds_read_b64_tr_b16 v[66:67], v12 offset:34816\n\t"
    "s_add_u32 m0, s81, 4096\n\t"
    "v_mfma_f32_16x16x32_bf16 v[124:127], v[72:75], a[244:247], v[124:127]\n\t"
    "ds_read_b64_tr_b16 v[68:69], v12 offset:36864\n\t"
    "global_load_lds_dwordx4 v2, s[68:69] offset:0\n\t"
    "v_mfma_f32_16x16x32_bf16 v[124:127], v[76:79], a[252:255], v[124:127]\n\t"
    "ds_read_b64_tr_b16 v[70:71], v12 offset:38912\n\t"
    "s_barrier\n\t"
    "s_waitcnt lgkmcnt(8)\n\t"
    "v_mfma_f32_16x16x32_bf16 v[224:227], v[48:51], v[128:131], 0\n\t"
    "ds_read_b64_tr_b16 v[72:73], v12 offset:49152\n\t"
    "v_mfma_f32_16x16x32_bf16 v[224:227], v[52:55], v[136:139], v[224:227]\n\t"
    "ds_read_b64_tr_b16 v[74:75], v12 offset:51200\n\t"
    "global_load_lds_dwordx4 v19, s[68:69] offset:1024\n\t"
    "v_mfma_f32_16x16x32_bf16 v[228:231], v[48:51], v[132:135], 0\n\t"
    "ds_read_b64_tr_b16 v[76:77], v12 offset:53248\n\t"
    "v_mfma_f32_16x16x32_bf16 v[228:231], v[52:55], v[140:143], v[228:231]\n\t"
    "ds_read_b64_tr_b16 v[78:79], v12 offset:55296\n\t"
    "s_waitcnt lgkmcnt(8)\n\t"
    "v_mfma_f32_16x16x32_bf16 v[232:235], v[56:59], v[128:131], 0\n\t"
    "ds_read_b64_tr_b16 v[48:49], v9 offset:40960\n\t"
    "s_add_u32 s54, s54, 229376\n\t"
    "s_addc_u32 s55, s55, 0\n\t"
    "v_mul_f32 v46, v80, v88\n\t"
    "v_mfma_f32_16x16x32_bf16 v[232:235], v[60:63], v[136:139], v[232:235]\n\t"
    "ds_read_b64_tr_b16 v[50:51], v9 offset:43008\n\t"
    "s_add_u32 m0, s77, 32768\n\t"
    "v_mfma_f32_16x16x32_bf16 v[236:239], v[56:59], v[132:135], 0\n\t"
    "ds_read_b64_tr_b16 v[52:53], v9 offset:45056\n\t"
    "global_load_lds_dwordx4 v6, s[54:55] offset:0\n\t"
    "v_mul_f32 v47, v81, v89\n\t"
    "v_mfma_f32_16x16x32_bf16 v[236:239], v[60:63], v[140:143], v[236:239]\n\t"
    "ds_read_b64_tr_b16 v[54:55], v9 offset:47104\n\t"
    "s_waitcnt lgkmcnt(8)\n\t"
    "v_mfma_f32_16x16x32_bf16 v[240:243], v[64:67], v[128:131], 0\n\t"
    "ds_read_b64_tr_b16 v[56:57], v9 offset:57344\n\t"
    "v_cvt_pk_bf16_f32 v24, v46, v47\n\t"
    "v_mfma_f32_16x16x32_bf16 v[240:243], v[68:71], v[136:139], v[240:243]\n\t"
    "ds_read_b64_tr_b16 v[58:59], v9 offset:59392\n\t"
    "global_load_lds_dwordx4 v10, s[54:55] offset:1024\n\t"
    "v_mfma_f32_16x16x32_bf16 v[244:247], v[64:67], v[132:135], 0\n\t"
    "ds_read_b64_tr_b16 v[60:61], v9 offset:61440\n\t"
    "v_mul_f32 v46, v82, v90\n\t"
    "v_mul_f32 v47, v83, v91\n\t"
    "v_mfma_f32_16x16x32_bf16 v[244:247], v[68:71], v[140:143], v[244:247]\n\t"
    "ds_read_b64_tr_b16 v[62:63], v9 offset:63488\n\t"
    "s_waitcnt lgkmcnt(8)\n\t"
    "v_mfma_f32_16x16x32_bf16 v[248:251], v[72:75], v[128:131], 0\n\t"
    "ds_read_b64_tr_b16 v[64:65], v12 offset:40960\n\t"
    "v_cvt_pk_bf16_f32 v25, v46, v47\n\t"
    "v_mfma_f32_16x16x32_bf16 v[248:251], v[76:79], v[136:139], v[248:251]\n\t"
    "ds_read_b64_tr_b16 v[66:67], v12 offset:43008\n\t"
    "global_load_lds_dwordx4 v13, s[54:55] offset:2048\n\t"
    "v_mfma_f32_16x16x32_bf16 v[252:255], v[72:75], v[132:135], 0\n\t"
    "ds_read_b64_tr_b16 v[68:69], v12 offset:45056\n\t"
    "v_mul_f32 v46, v96, v104\n\t"
    "v_mul_f32 v47, v97, v105\n\t"
    "v_mfma_f32_16x16x32_bf16 v[252:255], v[76:79], v[140:143], v[252:255]\n\t"
    "ds_read_b64_tr_b16 v[70:71], v12 offset:47104\n\t"
    "global_load_lds_dwordx4 v16, s[54:55] offset:3072\n\t"
    "s_waitcnt vmcnt(10)\n\t"
    "s_barrier\n\t"
    "s_waitcnt lgkmcnt(8)\n\t"
    "v_mfma_f32_16x16x32_bf16 v[224:227], v[48:51], v[144:147], v[224:227]\n\t"
    "ds_read_b64_tr_b16 v[72:73], v12 offset:57344\n\t"
    "v_cvt_pk_bf16_f32 v26, v46, v47\n\t"
    "v_mul_f32 v46, v98, v106\n\t"
    "v_mfma_f32_16x16x32_bf16 v[224:227], v[52:55], v[152:155], v[224:227]\n\t"
    "ds_read_b64_tr_b16 v[74:75], v12 offset:59392\n\t"
    "v_mul_f32 v47, v99, v107\n\t"
    "v_mfma_f32_16x16x32_bf16 v[228:231], v[48:51], v[148:151], v[228:231]\n\t"
    "ds_read_b64_tr_b16 v[76:77], v12 offset:61440\n\t"
    "v_cvt_pk_bf16_f32 v27, v46, v47\n\t"
    "global_store_dwordx4 v0, v[24:27], s[90:91] offset:0 sc1\n\t"
    "v_mfma_f32_16x16x32_bf16 v[228:231], v[52:55], v[156:159], v[228:231]\n\t"
    "ds_read_b64_tr_b16 v[78:79], v12 offset:63488\n\t"
    "s_waitcnt lgkmcnt(8)\n\t"
    "v_mfma_f32_16x16x32_bf16 v[232:235], v[56:59], v[144:147], v[232:235]\n\t"
    "ds_read_b64_tr_b16 v[48:49], v8 offset:0\n\t"
    "v_mul_f32 v46, v84, v92\n\t"
    "v_mul_f32 v47, v85, v93\n\t"
    "v_mfma_f32_16x16x32_bf16 v[232:235], v[60:63], v[152:155], v[232:235]\n\t"
    "ds_read_b64_tr_b16 v[50:51], v8 offset:2048\n\t"
    "v_cvt_pk_bf16_f32 v36, v46, v47\n\t"
    "v_mfma_f32_16x16x32_bf16 v[236:239], v[56:59], v[148:151], v[236:239]\n\t"
    "ds_read_b64_tr_b16 v[52:53], v8 offset:4096\n\t"
    "v_mul_f32 v46, v86, v94\n\t"
    "v_mul_f32 v47, v87, v95\n\t"
    "v_mfma_f32_16x16x32_bf16 v[236:239], v[60:63], v[156:159], v[236:239]\n\t"
    "ds_read_b64_tr_b16 v[54:55], v8 offset:6144\n\t"
    "s_waitcnt lgkmcnt(8)\n\t"
    "v_mfma_f32_16x16x32_bf16 v[240:243], v[64:67], v[144:147], v[240:243]\n\t"
    "ds_read_b64_tr_b16 v[56:57], v8 offset:49152\n\t"
    "s_add_u32 s54, s54, 229376\n\t"
    "s_addc_u32 s55, s55, 0\n\t"
    "v_cvt_pk_bf16_f32 v37, v46, v47\n\t"
    "v_mfma_f32_16x16x32_bf16 v[240:243], v[68:71], v[152:155], v[240:243]\n\t"
    "ds_read_b64_tr_b16 v[58:59], v8 offset:51200\n\t"
    "s_add_u32 m0, s77, 40960\n\t"
    "v_mfma_f32_16x16x32_bf16 v[244:247], v[64:67], v[148:151], v[244:247]\n\t"
    "ds_read_b64_tr_b16 v[60:61], v8 offset:53248\n\t"
    "global_load_lds_dwordx4 v6, s[54:55] offset:0\n\t"
    "v_mul_f32 v46, v100, v108\n\t"
    "v_mfma_f32_16x16x32_bf16 v[244:247], v[68:71], v[156:159], v[244:247]\n\t"
    "ds_read_b64_tr_b16 v[62:63], v8 offset:55296\n\t"
    "s_waitcnt lgkmcnt(8)\n\t"
    "v_mfma_f32_16x16x32_bf16 v[248:251], v[72:75], v[144:147], v[248:251]\n\t"
    "ds_read_b64_tr_b16 v[64:65], v11 offset:0\n\t"
    "v_mul_f32 v47, v101, v109\n\t"
    "v_mfma_f32_16x16x32_bf16 v[248:251], v[76:79], v[152:155], v[248:251]\n\t"
    "ds_read_b64_tr_b16 v[66:67], v11 offset:2048\n\t"
    "global_load_lds_dwordx4 v10, s[54:55] offset:1024\n\t"
    "v_mfma_f32_16x16x32_bf16 v[252:255], v[72:75], v[148:151], v[252:255]\n\t"
    "ds_read_b64_tr_b16 v[68:69], v11 offset:4096\n\t"
    "v_cvt_pk_bf16_f32 v38, v46, v47\n\t"
    "v_mul_f32 v46, v102, v110\n\t"
    "v_mfma_f32_16x16x32_bf16 v[252:255], v[76:79], v[156:159], v[252:255]\n\t"
    "ds_read_b64_tr_b16 v[70:71], v11 offset:6144\n\t"
    "s_waitcnt lgkmcnt(8)\n\t"
    "v_mfma_f32_16x16x32_bf16 v[224:227], v[48:51], v[160:163], v[224:227]\n\t"
    "ds_read_b64_tr_b16 v[72:73], v11 offset:49152\n\t"
    "v_mul_f32 v47, v103, v111\n\t"
    "v_mfma_f32_16x16x32_bf16 v[224:227], v[52:55], v[168:171], v[224:227]\n\t"
    "ds_read_b64_tr_b16 v[74:75], v11 offset:51200\n\t"
    "global_load_lds_dwordx4 v13, s[54:55] offset:2048\n\t"
    "v_mfma_f32_16x16x32_bf16 v[228:231], v[48:51], v[164:167], v[228:231]\n\t"
    "ds_read_b64_tr_b16 v[76:77], v11 offset:53248\n\t"
    "v_cvt_pk_bf16_f32 v39, v46, v47\n\t"
    "global_store_dwordx4 v0, v[36:39], s[94:95] offset:0 sc1\n\t"
    "v_mfma_f32_16x16x32_bf16 v[228:231], v[52:55], v[172:175], v[228:231]\n\t"
    "ds_read_b64_tr_b16 v[78:79], v11 offset:55296\n\t"
    "s_waitcnt lgkmcnt(8)\n\t"
    "v_mfma_f32_16x16x32_bf16 v[232:235], v[56:59], v[160:163], v[232:235]\n\t"
    "ds_read_b64_tr_b16 v[48:49], v8 offset:8192\n\t"
    "v_mul_f32 v46, v112, v120\n\t"
    "v_mul_f32 v47, v113, v121\n\t"
    "v_mfma_f32_16x16x32_bf16 v[232:235], v[60:63], v[168:171], v[232:235]\n\t"
    "ds_read_b64_tr_b16 v[50:51], v8 offset:10240\n\t"
    "v_cvt_pk_bf16_f32 v24, v46, v47\n\t"
    "v_mfma_f32_16x16x32_bf16 v[236:239], v[56:59], v[164:167], v[236:239]\n\t"
    "ds_read_b64_tr_b16 v[52:53], v8 offset:12288\n\t"
    "v_mul_f32 v46, v114, v122\n\t"
    "v_mul_f32 v47, v115, v123\n\t"
    "v_mfma_f32_16x16x32_bf16 v[236:239], v[60:63], v[172:175], v[236:239]\n\t"
    "ds_read_b64_tr_b16 v[54:55], v8 offset:14336\n\t"
    "s_waitcnt lgkmcnt(8)\n\t"
    "v_mfma_f32_16x16x32_bf16 v[240:243], v[64:67], v[160:163], v[240:243]\n\t"
    "ds_read_b64_tr_b16 v[56:57], v8 offset:57344\n\t"
    "v_cvt_pk_bf16_f32 v25, v46, v47\n\t"
    "v_mfma_f32_16x16x32_bf16 v[240:243], v[68:71], v[168:171], v[240:243]\n\t"
    "ds_read_b64_tr_b16 v[58:59], v8 offset:59392\n\t"
    "global_load_lds_dwordx4 v16, s[54:55] offset:3072\n\t"
    "v_mfma_f32_16x16x32_bf16 v[244:247], v[64:67], v[164:167], v[244:247]\n\t"
    "ds_read_b64_tr_b16 v[60:61], v8 offset:61440\n\t"
    "v_mul_f32 v46, v116, v124\n\t"
    "v_mul_f32 v47, v117, v125\n\t"
    "v_mfma_f32_16x16x32_bf16 v[244:247], v[68:71], v[172:175], v[244:247]\n\t"
    "ds_read_b64_tr_b16 v[62:63], v8 offset:63488\n\t"
    "s_waitcnt lgkmcnt(8)\n\t"
    "v_mfma_f32_16x16x32_bf16 v[248:251], v[72:75], v[160:163], v[248:251]\n\t"
    "ds_read_b64_tr_b16 v[64:65], v11 offset:8192\n\t"
    "v_cvt_pk_bf16_f32 v26, v46, v47\n\t"
    "v_mul_f32 v46, v118, v126\n\t"
    "v_mfma_f32_16x16x32_bf16 v[248:251], v[76:79], v[168:171], v[248:251]\n\t"
    "ds_read_b64_tr_b16 v[66:67], v11 offset:10240\n\t"
    "s_add_u32 s68, s86, 407552\n\t"
    "s_addc_u32 s69, s87, 0\n\t"
    "v_mfma_f32_16x16x32_bf16 v[252:255], v[72:75], v[164:167], v[252:255]\n\t"
    "ds_read_b64_tr_b16 v[68:69], v11 offset:12288\n\t"
    "s_add_u32 m0, s81, 4096\n\t"
    "v_mul_f32 v47, v119, v127\n\t"
    "v_mfma_f32_16x16x32_bf16 v[252:255], v[76:79], v[172:175], v[252:255]\n\t"
    "ds_read_b64_tr_b16 v[70:71], v11 offset:14336\n\t"
    "s_waitcnt lgkmcnt(8)\n\t"
    "v_mfma_f32_16x16x32_bf16 v[224:227], v[48:51], v[176:179], v[224:227]\n\t"
    "ds_read_b64_tr_b16 v[72:73], v11 offset:57344\n\t"
    "global_load_lds_dwordx4 v2, s[68:69] offset:2048\n\t"
    "v_cvt_pk_bf16_f32 v27, v46, v47\n\t"
    "v_mfma_f32_16x16x32_bf16 v[224:227], v[52:55], v[184:187], v[224:227]\n\t"
    "ds_read_b64_tr_b16 v[74:75], v11 offset:59392\n\t"
    "v_mfma_f32_16x16x32_bf16 v[228:231], v[48:51], v[180:183], v[228:231]\n\t"
    "ds_read_b64_tr_b16 v[76:77], v11 offset:61440\n\t"
    "global_load_lds_dwordx4 v19, s[68:69] offset:3072\n\t"
    "global_store_dwordx4 v0, v[24:27], s[42:43] sc1\n\t"
    "v_mfma_f32_16x16x32_bf16 v[228:231], v[52:55], v[188:191], v[228:231]\n\t"
    "ds_read_b64_tr_b16 v[78:79], v11 offset:63488\n\t"
    "s_waitcnt lgkmcnt(8)\n\t"
    "v_mfma_f32_16x16x32_bf16 v[232:235], v[56:59], v[176:179], v[232:235]\n\t"
    "ds_read_b64_tr_b16 v[48:49], v8 offset:16384\n\t"
    "s_add_u32 s62, s48, s70\n\t"
    "s_addc_u32 s63, s49, 0\n\t"
    "v_mfma_f32_16x16x32_bf16 v[232:235], v[60:63], v[184:187], v[232:235]\n\t"
    "ds_read_b64_tr_b16 v[50:51], v8 offset:18432\n\t"
    "s_add_u32 s66, s48, s71\n\t"
    "s_addc_u32 s67, s49, 0\n\t"
    "v_mfma_f32_16x16x32_bf16 v[236:239], v[56:59], v[180:183], v[236:239]\n\t"
    "ds_read_b64_tr_b16 v[52:53], v8 offset:20480\n\t"
    "s_add_u32 s96, s48, s72\n\t"
    "s_addc_u32 s97, s49, 0\n\t"
    "v_mfma_f32_16x16x32_bf16 v[236:239], v[60:63], v[188:191], v[236:239]\n\t"
    "ds_read_b64_tr_b16 v[54:55], v8 offset:22528\n\t"
    "s_waitcnt lgkmcnt(8)\n\t"
    "v_mfma_f32_16x16x32_bf16 v[240:243], v[64:67], v[176:179], v[240:243]\n\t"
    "ds_read_b64_tr_b16 v[56:57], v9 offset:0\n\t"
    "s_add_u32 s98, s48, s73\n\t"
    "s_addc_u32 s99, s49, 0\n\t"
    "v_mfma_f32_16x16x32_bf16 v[240:243], v[68:71], v[184:187], v[240:243]\n\t"
    "ds_read_b64_tr_b16 v[58:59], v9 offset:2048\n\t"
    "v_add_u32 v3, 6144, v0\n\t"
    "v_mfma_f32_16x16x32_bf16 v[244:247], v[64:67], v[180:183], v[244:247]\n\t"
    "ds_read_b64_tr_b16 v[60:61], v9 offset:4096\n\t"
    "s_add_u32 s42, s48, s61\n\t"
    "s_addc_u32 s43, s49, 0\n\t"
    "v_mfma_f32_16x16x32_bf16 v[244:247], v[68:71], v[188:191], v[244:247]\n\t"
    "ds_read_b64_tr_b16 v[62:63], v9 offset:6144\n\t"
    "s_waitcnt lgkmcnt(8)\n\t"
    "v_mfma_f32_16x16x32_bf16 v[248:251], v[72:75], v[176:179], v[248:251]\n\t"
    "ds_read_b64_tr_b16 v[64:65], v11 offset:16384\n\t"
    "s_add_u32 s44, s48, s47\n\t"
    "s_addc_u32 s45, s49, 0\n\t"
    "v_mfma_f32_16x16x32_bf16 v[248:251], v[76:79], v[184:187], v[248:251]\n\t"
    "ds_read_b64_tr_b16 v[66:67], v11 offset:18432\n\t"
    "s_and_b32 s84, s60, 1\n\t"
    "v_mfma_f32_16x16x32_bf16 v[252:255], v[72:75], v[180:183], v[252:255]\n\t"
    "ds_read_b64_tr_b16 v[68:69], v11 offset:20480\n\t"
    "s_lshl_b32 s84, s84, 10\n\t"
    "v_add_u32 v40, s84, v0\n\t"
    "v_mfma_f32_16x16x32_bf16 v[252:255], v[76:79], v[188:191], v[252:255]\n\t"
    "ds_read_b64_tr_b16 v[70:71], v11 offset:22528\n\t"
    "s_waitcnt lgkmcnt(8)\n\t"
    "v_mfma_f32_16x16x32_bf16 v[224:227], v[48:51], v[192:195], v[224:227]\n\t"
    "ds_read_b64_tr_b16 v[72:73], v12 offset:0\n\t"
    "v_xor_b32 v4, 0x400, v40\n\t"
    "s_waitcnt vmcnt(15)\n\t"
    "v_mfma_f32_16x16x32_bf16 v[224:227], v[52:55], v[200:203], v[224:227]\n\t"
    "ds_read_b64_tr_b16 v[74:75], v12 offset:2048\n\t"
    "ds_read_b64_tr_b16 a[128:129], v42 offset:0\n\t"
    "v_mfma_f32_16x16x32_bf16 v[228:231], v[48:51], v[196:199], v[228:231]\n\t"
    "ds_read_b64_tr_b16 v[76:77], v12 offset:4096\n\t"
    "ds_read_b64_tr_b16 a[130:131], v42 offset:2048\n\t"
    "ds_read_b64_tr_b16 a[132:133], v43 offset:0\n\t"
    "v_mfma_f32_16x16x32_bf16 v[228:231], v[52:55], v[204:207], v[228:231]\n\t"
    "ds_read_b64_tr_b16 a[134:135], v43 offset:2048\n\t"
    "s_waitcnt lgkmcnt(11)\n\t"
    "v_mfma_f32_16x16x32_bf16 v[232:235], v[56:59], v[192:195], v[232:235]\n\t"
    "ds_read_b64_tr_b16 v[78:79], v12 offset:6144\n\t"
    "ds_read_b64_tr_b16 a[136:137], v44 offset:0\n\t"
    "ds_read_b64_tr_b16 a[138:139], v44 offset:2048\n\t"
    "v_mfma_f32_16x16x32_bf16 v[232:235], v[60:63], v[200:203], v[232:235]\n\t"
    "ds_read_b64_tr_b16 a[140:141], v45 offset:0\n\t"
    "s_waitcnt lgkmcnt(13)\n\t"
    "ds_read_b64_tr_b16 a[142:143], v45 offset:2048\n\t"
    "v_mfma_f32_16x16x32_bf16 v[236:239], v[56:59], v[196:199], v[236:239]\n\t"
    "s_waitcnt lgkmcnt(0)\n\t"
    "v_mfma_f32_16x16x32_bf16 v[236:239], v[60:63], v[204:207], v[236:239]\n\t"
    "ds_read_b64_tr_b16 v[48:49], v8 offset:24576\n\t"
    "v_mfma_f32_16x16x32_bf16 v[240:243], v[64:67], v[192:195], v[240:243]\n\t"
    "ds_read_b64_tr_b16 v[50:51], v8 offset:26624\n\t"
    "s_add_u32 s68, s86, 32768\n\t"
    "s_addc_u32 s69, s87, 0\n\t"
    "v_mfma_f32_16x16x32_bf16 v[240:243], v[68:71], v[200:203], v[240:243]\n\t"
    "ds_read_b64_tr_b16 v[52:53], v8 offset:28672\n\t"
    "s_add_u32 m0, s81, 0\n\t"
    "v_mfma_f32_16x16x32_bf16 v[244:247], v[64:67], v[196:199], v[244:247]\n\t"
    "ds_read_b64_tr_b16 v[54:55], v8 offset:30720\n\t"
    "global_load_lds_dwordx4 v2, s[68:69] offset:0\n\t"
    "v_mfma_f32_16x16x32_bf16 v[244:247], v[68:71], v[204:207], v[244:247]\n\t"
    "ds_read_b64_tr_b16 v[56:57], v9 offset:8192\n\t"
    "v_mfma_f32_16x16x32_bf16 v[248:251], v[72:75], v[192:195], v[248:251]\n\t"
    "ds_read_b64_tr_b16 v[58:59], v9 offset:10240\n\t"
    "v_mfma_f32_16x16x32_bf16 v[248:251], v[76:79], v[200:203], v[248:251]\n\t"
    "ds_read_b64_tr_b16 v[60:61], v9 offset:12288\n\t"
    "global_load_lds_dwordx4 v19, s[68:69] offset:1024\n\t"
    "v_mfma_f32_16x16x32_bf16 v[252:255], v[72:75], v[196:199], v[252:255]\n\t"
    "ds_read_b64_tr_b16 v[62:63], v9 offset:14336\n\t"
    "v_mfma_f32_16x16x32_bf16 v[252:255], v[76:79], v[204:207], v[252:255]\n\t"
    "ds_read_b64_tr_b16 v[64:65], v11 offset:24576\n\t"
    "s_add_u32 s68, s86, 47104\n\t"
    "s_addc_u32 s69, s87, 0\n\t"
    "global_load_lds_dwordx4 v2, s[68:69] offset:2048\n\t"
    "s_waitcnt vmcnt(6)\n\t"
    "s_barrier\n\t"
    "s_waitcnt lgkmcnt(5)\n\t"
    "v_mfma_f32_16x16x32_bf16 v[224:227], v[48:51], v[208:211], v[224:227]\n\t"
    "ds_read_b64_tr_b16 v[66:67], v11 offset:26624\n\t"
    "v_mfma_f32_16x16x32_bf16 v[224:227], v[52:55], v[216:219], v[224:227]\n\t"
    "ds_read_b64_tr_b16 v[68:69], v11 offset:28672\n\t"
    "v_mfma_f32_16x16x32_bf16 v[228:231], v[48:51], v[212:215], v[228:231]\n\t"
    "ds_read_b64_tr_b16 v[70:71], v11 offset:30720\n\t"
    "v_mfma_f32_16x16x32_bf16 v[228:231], v[52:55], v[220:223], v[228:231]\n\t"
    "ds_read_b64_tr_b16 v[72:73], v12 offset:8192\n\t"
    "s_waitcnt lgkmcnt(5)\n\t"
    "v_mfma_f32_16x16x32_bf16 v[232:235], v[56:59], v[208:211], v[232:235]\n\t"
    "ds_read_b64_tr_b16 v[74:75], v12 offset:10240\n\t"
    "v_mfma_f32_16x16x32_bf16 v[232:235], v[60:63], v[216:219], v[232:235]\n\t"
    "ds_read_b64_tr_b16 v[76:77], v12 offset:12288\n\t"
    "global_load_lds_dwordx4 v19, s[68:69] offset:3072\n\t"
    "v_mfma_f32_16x16x32_bf16 v[236:239], v[56:59], v[212:215], v[236:239]\n\t"
    "ds_read_b64_tr_b16 v[78:79], v12 offset:14336\n\t"
    "s_waitcnt vmcnt(5)\n\t"
    "ds_read_b64_tr_b16 a[144:145], v42 offset:4096\n\t"
    "v_mfma_f32_16x16x32_bf16 v[236:239], v[60:63], v[220:223], v[236:239]\n\t"
    "ds_read_b64_tr_b16 v[48:49], v8 offset:32768\n\t"
    "s_waitcnt lgkmcnt(6)\n\t"
    "v_mfma_f32_16x16x32_bf16 v[240:243], v[64:67], v[208:211], v[240:243]\n\t"
    "ds_read_b64_tr_b16 v[50:51], v8 offset:34816\n\t"
    "ds_read_b64_tr_b16 a[146:147], v42 offset:6144\n\t"
    "ds_read_b64_tr_b16 a[148:149], v43 offset:4096\n\t"
    "v_mfma_f32_16x16x32_bf16 v[240:243], v[68:71], v[216:219], v[240:243]\n\t"
    "ds_read_b64_tr_b16 v[52:53], v8 offset:36864\n\t"
    "ds_read_b64_tr_b16 a[150:151], v43 offset:6144\n\t"
    "v_mfma_f32_16x16x32_bf16 v[244:247], v[64:67], v[212:215], v[244:247]\n\t"
    "ds_read_b64_tr_b16 v[54:55], v8 offset:38912\n\t"
    "ds_read_b64_tr_b16 a[152:153], v44 offset:4096\n\t"
    "ds_read_b64_tr_b16 a[154:155], v44 offset:6144\n\t"
    "v_mfma_f32_16x16x32_bf16 v[244:247], v[68:71], v[220:223], v[244:247]\n\t"
    "ds_read_b64_tr_b16 a[156:157], v45 offset:4096\n\t"
    "s_waitcnt lgkmcnt(11)\n\t"
    "v_mfma_f32_16x16x32_bf16 v[248:251], v[72:75], v[208:211], v[248:251]\n\t"
    "ds_read_b64_tr_b16 v[56:57], v9 offset:16384\n\t"
    "s_waitcnt vmcnt(4)\n\t"
    "s_add_u32 s89, s74, 0\n\t"
    "s_mov_b64 exec, 1\n\t"
    "buffer_atomic_swap_x2 v[32:33], v0, %[sw], s89 offen\n\t"
    "s_mov_b64 exec, -1\n\t"
    "v_mfma_f32_16x16x32_bf16 v[248:251], v[76:79], v[216:219], v[248:251]\n\t"
    "ds_read_b64_tr_b16 v[58:59], v9 offset:18432\n\t"
    "ds_read_b64_tr_b16 a[158:159], v45 offset:6144\n\t"
    "v_mfma_f32_16x16x32_bf16 v[252:255], v[72:75], v[212:215], v[252:255]\n\t"
    "s_waitcnt lgkmcnt(0)\n\t"
    "v_mfma_f32_16x16x32_bf16 v[252:255], v[76:79], v[220:223], v[252:255]\n\t"
    "ds_read_b64_tr_b16 v[60:61], v9 offset:20480\n\t"
    "v_mfma_f32_16x16x32_bf16 v[224:227], v[48:51], a[224:227], v[224:227]\n\t"
    "ds_read_b64_tr_b16 v[62:63], v9 offset:22528\n\t"
    "s_add_u32 s68, s86, 163840\n\t"
    "s_addc_u32 s69, s87, 0\n\t"
    "v_mfma_f32_16x16x32_bf16 v[224:227], v[52:55], a[232:235], v[224:227]\n\t"
    "ds_read_b64_tr_b16 v[64:65], v11 offset:32768\n\t"
    "s_add_u32 m0, s81, 4096\n\t"
    "v_mfma_f32_16x16x32_bf16 v[228:231], v[48:51], a[228:231], v[228:231]\n\t"
    "ds_read_b64_tr_b16 v[66:67], v11 offset:34816\n\t"
    "global_load_lds_dwordx4 v2, s[68:69] offset:0\n\t"
    "v_mfma_f32_16x16x32_bf16 v[228:231], v[52:55], a[236:239], v[228:231]\n\t"
    "ds_read_b64_tr_b16 v[68:69], v11 offset:36864\n\t"
    "s_waitcnt lgkmcnt(3)\n\t"
    "v_mfma_f32_16x16x32_bf16 v[232:235], v[56:59], a[224:227], v[232:235]\n\t"
    "ds_read_b64_tr_b16 v[70:71], v11 offset:38912\n\t"
    "v_mfma_f32_16x16x32_bf16 v[232:235], v[60:63], a[232:235], v[232:235]\n\t"
    "ds_read_b64_tr_b16 v[72:73], v12 offset:16384\n\t"
    "global_load_lds_dwordx4 v19, s[68:69] offset:1024\n\t"
    "v_mfma_f32_16x16x32_bf16 v[236:239], v[56:59], a[228:231], v[236:239]\n\t"
    "ds_read_b64_tr_b16 v[74:75], v12 offset:18432\n\t"
    "v_mfma_f32_16x16x32_bf16 v[236:239], v[60:63], a[236:239], v[236:239]\n\t"
    "ds_read_b64_tr_b16 v[76:77], v12 offset:20480\n\t"
    "s_waitcnt lgkmcnt(3)\n\t"
    "v_mfma_f32_16x16x32_bf16 v[240:243], v[64:67], a[224:227], v[240:243]\n\t"
    "ds_read_b64_tr_b16 v[78:79], v12 offset:22528\n\t"
    "s_add_u32 s68, s86, 178176\n\t"
    "s_addc_u32 s69, s87, 0\n\t"
    "v_mfma_f32_16x16x32_bf16 v[240:243], v[68:71], a[232:235], v[240:243]\n\t"
    "ds_read_b64_tr_b16 v[48:49], v8 offset:40960\n\t"
    "v_mfma_f32_16x16x32_bf16 v[244:247], v[64:67], a[228:231], v[244:247]\n\t"
    "ds_read_b64_tr_b16 v[50:51], v8 offset:43008\n\t"
    "global_load_lds_dwordx4 v2, s[68:69] offset:2048\n\t"
    "v_mfma_f32_16x16x32_bf16 v[244:247], v[68:71], a[236:239], v[244:247]\n\t"
    "ds_read_b64_tr_b16 v[52:53], v8 offset:45056\n\t"
    "s_waitcnt lgkmcnt(3)\n\t"
    "v_mfma_f32_16x16x32_bf16 v[248:251], v[72:75], a[224:227], v[248:251]\n\t"
    "ds_read_b64_tr_b16 v[54:55], v8 offset:47104\n\t"
    "v_mfma_f32_16x16x32_bf16 v[248:251], v[76:79], a[232:235], v[248:251]\n\t"
    "ds_read_b64_tr_b16 v[56:57], v9 offset:24576\n\t"
    "global_load_lds_dwordx4 v19, s[68:69] offset:3072\n\t"
    "v_mfma_f32_16x16x32_bf16 v[252:255], v[72:75], a[228:231], v[252:255]\n\t"
    "ds_read_b64_tr_b16 v[58:59], v9 offset:26624\n\t"
    "v_mfma_f32_16x16x32_bf16 v[252:255], v[76:79], a[236:239], v[252:255]\n\t"
    "ds_read_b64_tr_b16 v[60:61], v9 offset:28672\n\t"
    "s_waitcnt lgkmcnt(3)\n\t"
    "v_mfma_f32_16x16x32_bf16 v[224:227], v[48:51], a[240:243], v[224:227]\n\t"
    "ds_read_b64_tr_b16 v[62:63], v9 offset:30720\n\t"
    "v_mfma_f32_16x16x32_bf16 v[224:227], v[52:55], a[248:251], v[224:227]\n\t"
    "ds_read_b64_tr_b16 v[64:65], v11 offset:40960\n\t"
    "v_mfma_f32_16x16x32_bf16 v[228:231], v[48:51], a[244:247], v[228:231]\n\t"
    "ds_read_b64_tr_b16 v[66:67], v11 offset:43008\n\t"
    "v_mfma_f32_16x16x32_bf16 v[228:231], v[52:55], a[252:255], v[228:231]\n\t"
    "ds_read_b64_tr_b16 v[68:69], v11 offset:45056\n\t"
    "s_waitcnt lgkmcnt(3)\n\t"
    "v_mfma_f32_16x16x32_bf16 v[232:235], v[56:59], a[240:243], v[232:235]\n\t"
    "ds_read_b64_tr_b16 v[70:71], v11 offset:47104\n\t"
    "v_mfma_f32_16x16x32_bf16 v[232:235], v[60:63], a[248:251], v[232:235]\n\t"
    "ds_read_b64_tr_b16 v[72:73], v12 offset:24576\n\t"
    "s_add_u32 s89, s75, 0\n\t"
    "v_mov_b64 v[30:31], s[56:57]\n\t"
    "s_mov_b64 exec, 0xff\n\t"
    "v_mov_b64 v[30:31], 0\n\t"
    "buffer_atomic_or_x2 v[30:31], v20, %[sw], s89 offen sc0\n\t"
    "s_mov_b64 exec, -1\n\t"
    "v_mfma_f32_16x16x32_bf16 v[236:239], v[56:59], a[244:247], v[236:239]\n\t"
    "ds_read_b64_tr_b16 v[74:75], v12 offset:26624\n\t"
    "v_mfma_f32_16x16x32_bf16 v[236:239], v[60:63], a[252:255], v[236:239]\n\t"
    "ds_read_b64_tr_b16 v[76:77], v12 offset:28672\n\t"
    "s_waitcnt lgkmcnt(3)\n\t"
    "v_mfma_f32_16x16x32_bf16 v[240:243], v[64:67], a[240:243], v[240:243]\n\t"
    "ds_read_b64_tr_b16 v[78:79], v12 offset:30720\n\t"
    "v_mfma_f32_16x16x32_bf16 v[240:243], v[68:71], a[248:251], v[240:243]\n\t"
    "ds_read_b64_tr_b16 v[48:49], v15 offset:32768\n\t"
    "s_waitcnt vmcnt(6)\n\t"
    "ds_read_b64_tr_b16 a[160:161], v42 offset:0\n\t"
    "v_mfma_f32_16x16x32_bf16 v[244:247], v[64:67], a[244:247], v[244:247]\n\t"
    "ds_read_b64_tr_b16 v[50:51], v15 offset:34816\n\t"
    "ds_read_b64_tr_b16 a[162:163], v42 offset:2048\n\t"
    "v_mfma_f32_16x16x32_bf16 v[244:247], v[68:71], a[252:255], v[244:247]\n\t"
    "ds_read_b64_tr_b16 v[52:53], v15 offset:36864\n\t"
    "ds_read_b64_tr_b16 a[164:165], v43 offset:0\n\t"
    "s_waitcnt lgkmcnt(6)\n\t"
    "v_mfma_f32_16x16x32_bf16 v[248:251], v[72:75], a[240:243], v[248:251]\n\t"
    "ds_read_b64_tr_b16 v[54:55], v15 offset:38912\n\t"
    "ds_read_b64_tr_b16 a[166:167], v43 offset:2048\n\t"
    "v_mfma_f32_16x16x32_bf16 v[248:251], v[76:79], a[248:251], v[248:251]\n\t"
    "ds_read_b64_tr_b16 v[56:57], v15 offset:49152\n\t"
    "ds_read_b64_tr_b16 a[168:169], v44 offset:0\n\t"
    "ds_read_b64_tr_b16 a[170:171], v44 offset:2048\n\t"
    "v_mfma_f32_16x16x32_bf16 v[252:255], v[72:75], a[244:247], v[252:255]\n\t"
    "ds_read_b64_tr_b16 v[58:59], v15 offset:51200\n\t"
    "ds_read_b64_tr_b16 a[172:173], v45 offset:0\n\t"
    "v_mfma_f32_16x16x32_bf16 v[252:255], v[76:79], a[252:255], v[252:255]\n\t"
    "ds_read_b64_tr_b16 v[60:61], v15 offset:53248\n\t"
    "ds_read_b64_tr_b16 a[174:175], v45 offset:2048\n\t"
    "s_waitcnt lgkmcnt(8)\n\t"
    "v_mfma_f32_16x16x32_bf16 v[80:83], v[48:51], v[128:131], 0\n\t"
    "ds_read_b64_tr_b16 v[62:63], v15 offset:55296\n\t"
    "s_waitcnt lgkmcnt(1)\n\t"
    "v_mfma_f32_16x16x32_bf16 v[80:83], v[52:55], v[136:139], v[80:83]\n\t"
    "ds_read_b64_tr_b16 v[64:65], v18 offset:32768\n\t"
    "s_add_u32 s68, s86, 294912\n\t"
    "s_addc_u32 s69, s87, 0\n\t"
    "v_mfma_f32_16x16x32_bf16 v[84:87], v[48:51], v[132:135], 0\n\t"
    "ds_read_b64_tr_b16 v[66:67], v18 offset:34816\n\t"
    "s_add_u32 m0, s81, 0\n\t"
    "v_mfma_f32_16x16x32_bf16 v[84:87], v[52:55], v[140:143], v[84:87]\n\t"
    "ds_read_b64_tr_b16 v[68:69], v18 offset:36864\n\t"
    "s_waitcnt lgkmcnt(3)\n\t"
    "v_mfma_f32_16x16x32_bf16 v[88:91], v[56:59], v[128:131], 0\n\t"
    "ds_read_b64_tr_b16 v[70:71], v18 offset:38912\n\t"
    "global_load_lds_dwordx4 v2, s[68:69] offset:0\n\t"
    "v_mul_f32 v46, v224, v232\n\t"
    "v_mfma_f32_16x16x32_bf16 v[88:91], v[60:63], v[136:139], v[88:91]\n\t"
    "ds_read_b64_tr_b16 v[72:73], v18 offset:49152\n\t"
    "v_mfma_f32_16x16x32_bf16 v[92:95], v[56:59], v[132:135], 0\n\t"
    "ds_read_b64_tr_b16 v[74:75], v18 offset:51200\n\t"
    "global_load_lds_dwordx4 v19, s[68:69] offset:1024\n\t"
    "v_mul_f32 v47, v225, v233\n\t"
    "v_mfma_f32_16x16x32_bf16 v[92:95], v[60:63], v[140:143], v[92:95]\n\t"
    "ds_read_b64_tr_b16 v[76:77], v18 offset:53248\n\t"
    "s_waitcnt lgkmcnt(3)\n\t"
    "v_mfma_f32_16x16x32_bf16 v[96:99], v[64:67], v[128:131], 0\n\t"
    "ds_read_b64_tr_b16 v[78:79], v18 offset:55296\n\t"
    "v_cvt_pk_bf16_f32 v24, v46, v47\n\t"
    "v_mul_f32 v46, v226, v234\n\t"
    "v_mfma_f32_16x16x32_bf16 v[96:99], v[68:71], v[136:139], v[96:99]\n\t"
    "ds_read_b64_tr_b16 v[48:49], v15 offset:40960\n\t"
    "s_add_u32 s68, s86, 309248\n\t"
    "s_addc_u32 s69, s87, 0\n\t"
    "v_mfma_f32_16x16x32_bf16 v[100:103], v[64:67], v[132:135], 0\n\t"
    "ds_read_b64_tr_b16 v[50:51], v15 offset:43008\n\t"
    "v_mul_f32 v47, v227, v235\n\t"
    "v_mfma_f32_16x16x32_bf16 v[100:103], v[68:71], v[140:143], v[100:103]\n\t"
    "ds_read_b64_tr_b16 v[52:53], v15 offset:45056\n\t"
    "s_waitcnt lgkmcnt(3)\n\t"
    "v_mfma_f32_16x16x32_bf16 v[104:107], v[72:75], v[128:131], 0\n\t"
    "ds_read_b64_tr_b16 v[54:55], v15 offset:47104\n\t"
    "global_load_lds_dwordx4 v2, s[68:69] offset:2048\n\t"
    "v_cvt_pk_bf16_f32 v25, v46, v47\n\t"
    "v_mfma_f32_16x16x32_bf16 v[104:107], v[76:79], v[136:139], v[104:107]\n\t"
    "ds_read_b64_tr_b16 v[56:57], v15 offset:57344\n\t"
    "v_mfma_f32_16x16x32_bf16 v[108:111], v[72:75], v[132:135], 0\n\t"
    "ds_read_b64_tr_b16 v[58:59], v15 offset:59392\n\t"
    "global_load_lds_dwordx4 v19, s[68:69] offset:3072\n\t"
    "v_mul_f32 v46, v240, v248\n\t"
    "v_mfma_f32_16x16x32_bf16 v[108:111], v[76:79], v[140:143], v[108:111]\n\t"
    "ds_read_b64_tr_b16 v[60:61], v15 offset:61440\n\t"
    "s_waitcnt lgkmcnt(3)\n\t"
    "v_mfma_f32_16x16x32_bf16 v[80:83], v[48:51], v[144:147], v[80:83]\n\t"
    "ds_read_b64_tr_b16 v[62:63], v15 offset:63488\n\t"
    "v_mul_f32 v47, v241, v249\n\t"
    "v_cvt_pk_bf16_f32 v26, v46, v47\n\t"
    "v_mfma_f32_16x16x32_bf16 v[80:83], v[52:55], v[152:155], v[80:83]\n\t"
    "ds_read_b64_tr_b16 v[64:65], v18 offset:40960\n\t"
    "v_mul_f32 v46, v242, v250\n\t"
    "v_mfma_f32_16x16x32_bf16 v[84:87], v[48:51], v[148:151], v[84:87]\n\t"
    "ds_read_b64_tr_b16 v[66:67], v18 offset:43008\n\t"
    "v_mul_f32 v47, v243, v251\n\t"
    "v_cvt_pk_bf16_f32 v27, v46, v47\n\t"
    "v_mfma_f32_16x16x32_bf16 v[84:87], v[52:55], v[156:159], v[84:87]\n\t"
    "ds_read_b64_tr_b16 v[68:69], v18 offset:45056\n\t"
    "s_waitcnt lgkmcnt(3)\n\t"
    "v_mfma_f32_16x16x32_bf16 v[88:91], v[56:59], v[144:147], v[88:91]\n\t"
    "ds_read_b64_tr_b16 v[70:71], v18 offset:47104\n\t"
    "global_store_dwordx4 v0, v[24:27], s[90:91] offset:1024 sc1\n\t"
    "v_mul_f32 v46, v228, v236\n\t"
    "v_mfma_f32_16x16x32_bf16 v[88:91], v[60:63], v[152:155], v[88:91]\n\t"
    "ds_read_b64_tr_b16 v[72:73], v18 offset:57344\n\t"
    "v_mul_f32 v47, v229, v237\n\t"
    "v_mfma_f32_16x16x32_bf16 v[92:95], v[56:59], v[148:151], v[92:95]\n\t"
    "ds_read_b64_tr_b16 v[74:75], v18 offset:59392\n\t"
    "v_cvt_pk_bf16_f32 v36, v46, v47\n\t"
    "v_mul_f32 v46, v230, v238\n\t"
    "v_mfma_f32_16x16x32_bf16 v[92:95], v[60:63], v[156:159], v[92:95]\n\t"
    "ds_read_b64_tr_b16 v[76:77], v18 offset:61440\n\t"
    "s_waitcnt lgkmcnt(3)\n\t"
    "v_mfma_f32_16x16x32_bf16 v[96:99], v[64:67], v[144:147], v[96:99]\n\t"
    "ds_read_b64_tr_b16 v[78:79], v18 offset:63488\n\t"
    "v_mul_f32 v47, v231, v239\n\t"
    "v_cvt_pk_bf16_f32 v37, v46, v47\n\t"
    "v_mfma_f32_16x16x32_bf16 v[96:99], v[68:71], v[152:155], v[96:99]\n\t"
    "ds_read_b64_tr_b16 v[48:49], v14 offset:0\n\t"
    "v_mul_f32 v46, v244, v252\n\t"
    "v_mfma_f32_16x16x32_bf16 v[100:103], v[64:67], v[148:151], v[100:103]\n\t"
    "ds_read_b64_tr_b16 v[50:51], v14 offset:2048\n\t"
    "v_mul_f32 v47, v245, v253\n\t"
    "v_cvt_pk_bf16_f32 v38, v46, v47\n\t"
    "v_mfma_f32_16x16x32_bf16 v[100:103], v[68:71], v[156:159], v[100:103]\n\t"
    "ds_read_b64_tr_b16 v[52:53], v14 offset:4096\n\t"
    "v_mul_f32 v46, v246, v254\n\t"
    "v_mul_f32 v47, v247, v255\n\t"
    "v_cvt_pk_bf16_f32 v39, v46, v47\n\t"
    "global_store_dwordx4 v0, v[36:39], s[94:95] offset:1024 sc1\n\t"
    "v_lshrrev_b32 v41, 2, v0\n\t"
    "s_lshr_b32 s84, s81, 5\n\t"
    "v_add_u32 v41, s84, v41\n\t"
    "v_add_u32 v41, 45056, v41\n\t"
    "s_waitcnt vmcnt(6)\n\t"
    "v_cmp_ne_u64 vcc, v[30:31], s[56:57]\n\t"
    "s_cmp_eq_u64 vcc, 0\n\t"
    "s_cbranch_scc1 91f\n\t"
    "s_mov_b32 s88, 0x40000\n\t"
    "90:\n\t"
    "s_add_u32 s89, s75, 0\n\t"
    "v_mov_b64 v[30:31], s[56:57]\n\t"
    "s_mov_b64 exec, 0xff\n\t"
    "v_mov_b64 v[30:31], 0\n\t"
    "buffer_atomic_or_x2 v[30:31], v20, %[sw], s89 offen sc0\n\t"
    "s_mov_b64 exec, -1\n\t"
    "s_waitcnt vmcnt(0)\n\t"
    "v_cmp_ne_u64 vcc, v[30:31], s[56:57]\n\t"
    "s_cmp_eq_u64 vcc, 0\n\t"
    "s_cbranch_scc1 91f\n\t"
    "s_sub_u32 s88, s88, 1\n\t"
    "s_cmp_lg_u32 s88, 0\n\t"
    "s_cbranch_scc1 90b\n\t"
    "91:\n\t"
    "s_waitcnt lgkmcnt(3)\n\t"
    "v_mfma_f32_16x16x32_bf16 v[104:107], v[72:75], v[144:147], v[104:107]\n\t"
    "ds_read_b64_tr_b16 v[54:55], v14 offset:6144\n\t"
    "v_mfma_f32_16x16x32_bf16 v[104:107], v[76:79], v[152:155], v[104:107]\n\t"
    "ds_read_b64_tr_b16 v[56:57], v14 offset:49152\n\t"
    "v_mfma_f32_16x16x32_bf16 v[108:111], v[72:75], v[148:151], v[108:111]\n\t"
    "ds_read_b64_tr_b16 v[58:59], v14 offset:51200\n\t"
    "v_mfma_f32_16x16x32_bf16 v[108:111], v[76:79], v[156:159], v[108:111]\n\t"
    "ds_read_b64_tr_b16 v[60:61], v14 offset:53248\n\t"
    "s_waitcnt lgkmcnt(3)\n\t"
    "v_mfma_f32_16x16x32_bf16 v[80:83], v[48:51], v[160:163], v[80:83]\n\t"
    "ds_read_b64_tr_b16 v[62:63], v14 offset:55296\n\t"
    "global_load_dwordx4 a[192:195], v0, s[62:63] offset:-3072 sc1\n\t"
    "v_mfma_f32_16x16x32_bf16 v[80:83], v[52:55], v[168:171], v[80:83]\n\t"
    "ds_read_b64_tr_b16 v[64:65], v17 offset:0\n\t"
    "v_mfma_f32_16x16x32_bf16 v[84:87], v[48:51], v[164:167], v[84:87]\n\t"
    "ds_read_b64_tr_b16 v[66:67], v17 offset:2048\n\t"
    "v_mfma_f32_16x16x32_bf16 v[84:87], v[52:55], v[172:175], v[84:87]\n\t"
    "ds_read_b64_tr_b16 v[68:69], v17 offset:4096\n\t"
    "s_waitcnt lgkmcnt(3)\n\t"
    "v_mfma_f32_16x16x32_bf16 v[88:91], v[56:59], v[160:163], v[88:91]\n\t"
    "ds_read_b64_tr_b16 v[70:71], v17 offset:6144\n\t"
    "global_load_dwordx4 a[196:199], v0, s[66:67] offset:-3072 sc1\n\t"
    "v_mfma_f32_16x16x32_bf16 v[88:91], v[60:63], v[168:171], v[88:91]\n\t"
    "ds_read_b64_tr_b16 v[72:73], v17 offset:49152\n\t"
    "v_mfma_f32_16x16x32_bf16 v[92:95], v[56:59], v[164:167], v[92:95]\n\t"
    "ds_read_b64_tr_b16 v[74:75], v17 offset:51200\n\t"
    "v_mfma_f32_16x16x32_bf16 v[92:95], v[60:63], v[172:175], v[92:95]\n\t"
    "ds_read_b64_tr_b16 v[76:77], v17 offset:53248\n\t"
    "s_waitcnt lgkmcnt(3)\n\t"
    "v_mfma_f32_16x16x32_bf16 v[96:99], v[64:67], v[160:163], v[96:99]\n\t"
    "ds_read_b64_tr_b16 v[78:79], v17 offset:55296\n\t"
    "global_load_dwordx4 a[200:203], v0, s[96:97] offset:-3072 sc1\n\t"
    "v_mfma_f32_16x16x32_bf16 v[96:99], v[68:71], v[168:171], v[96:99]\n\t"
    "ds_read_b64_tr_b16 v[48:49], v14 offset:8192\n\t"
    "v_mfma_f32_16x16x32_bf16 v[100:103], v[64:67], v[164:167], v[100:103]\n\t"
    "ds_read_b64_tr_b16 v[50:51], v14 offset:10240\n\t"
    "v_mfma_f32_16x16x32_bf16 v[100:103], v[68:71], v[172:175], v[100:103]\n\t"
    "ds_read_b64_tr_b16 v[52:53], v14 offset:12288\n\t"
    "s_waitcnt lgkmcnt(3)\n\t"
    "v_mfma_f32_16x16x32_bf16 v[104:107], v[72:75], v[160:163], v[104:107]\n\t"
    "ds_read_b64_tr_b16 v[54:55], v14 offset:14336\n\t"
    "global_load_dwordx4 a[204:207], v0, s[98:99] offset:-3072 sc1\n\t"
    "v_mfma_f32_16x16x32_bf16 v[104:107], v[76:79], v[168:171], v[104:107]\n\t"
    "ds_read_b64_tr_b16 v[56:57], v14 offset:57344\n\t"
    "v_mfma_f32_16x16x32_bf16 v[108:111], v[72:75], v[164:167], v[108:111]\n\t"
    "ds_read_b64_tr_b16 v[58:59], v14 offset:59392\n\t"
    "v_mfma_f32_16x16x32_bf16 v[108:111], v[76:79], v[172:175], v[108:111]\n\t"
    "ds_read_b64_tr_b16 v[60:61], v14 offset:61440\n\t"
    "s_waitcnt lgkmcnt(3)\n\t"
    "v_mfma_f32_16x16x32_bf16 v[80:83], v[48:51], v[176:179], v[80:83]\n\t"
    "ds_read_b64_tr_b16 v[62:63], v14 offset:63488\n\t"
    "global_load_dwordx4 a[208:211], v0, s[62:63] offset:0 sc1\n\t"
    "v_mfma_f32_16x16x32_bf16 v[80:83], v[52:55], v[184:187], v[80:83]\n\t"
    "ds_read_b64_tr_b16 v[64:65], v17 offset:8192\n\t"
    "v_mfma_f32_16x16x32_bf16 v[84:87], v[48:51], v[180:183], v[84:87]\n\t"
    "ds_read_b64_tr_b16 v[66:67], v17 offset:10240\n\t"
    "v_mfma_f32_16x16x32_bf16 v[84:87], v[52:55], v[188:191], v[84:87]\n\t"
    "ds_read_b64_tr_b16 v[68:69], v17 offset:12288\n\t"
    "s_waitcnt lgkmcnt(3)\n\t"
    "v_mfma_f32_16x16x32_bf16 v[88:91], v[56:59], v[176:179], v[88:91]\n\t"
    "ds_read_b64_tr_b16 v[70:71], v17 offset:14336\n\t"
    "global_load_dwordx4 a[212:215], v0, s[66:67] offset:0 sc1\n\t"
    "v_mfma_f32_16x16x32_bf16 v[88:91], v[60:63], v[184:187], v[88:91]\n\t"
    "ds_read_b64_tr_b16 v[72:73], v17 offset:57344\n\t"
    "v_mfma_f32_16x16x32_bf16 v[92:95], v[56:59], v[180:183], v[92:95]\n\t"
    "ds_read_b64_tr_b16 v[74:75], v17 offset:59392\n\t"
    "v_mfma_f32_16x16x32_bf16 v[92:95], v[60:63], v[188:191], v[92:95]\n\t"
    "ds_read_b64_tr_b16 v[76:77], v17 offset:61440\n\t"
    "s_waitcnt lgkmcnt(3)\n\t"
    "v_mfma_f32_16x16x32_bf16 v[96:99], v[64:67], v[176:179], v[96:99]\n\t"
    "ds_read_b64_tr_b16 v[78:79], v17 offset:63488\n\t"
    "global_load_dwordx4 a[216:219], v0, s[96:97] offset:0 sc1\n\t"
    "v_mfma_f32_16x16x32_bf16 v[96:99], v[68:71], v[184:187], v[96:99]\n\t"
    "ds_read_b64_tr_b16 v[48:49], v14 offset:16384\n\t"
    "v_mfma_f32_16x16x32_bf16 v[100:103], v[64:67], v[180:183], v[100:103]\n\t"
    "ds_read_b64_tr_b16 v[50:51], v14 offset:18432\n\t"
    "v_mfma_f32_16x16x32_bf16 v[100:103], v[68:71], v[188:191], v[100:103]\n\t"
    "ds_read_b64_tr_b16 v[52:53], v14 offset:20480\n\t"
    "s_waitcnt lgkmcnt(3)\n\t"
    "v_mfma_f32_16x16x32_bf16 v[104:107], v[72:75], v[176:179], v[104:107]\n\t"
    "ds_read_b64_tr_b16 v[54:55], v14 offset:22528\n\t"
    "global_load_dwordx4 a[220:223], v0, s[98:99] offset:0 sc1\n\t"
    "v_mfma_f32_16x16x32_bf16 v[104:107], v[76:79], v[184:187], v[104:107]\n\t"
    "ds_read_b64_tr_b16 v[56:57], v15 offset:0\n\t"
    "v_mfma_f32_16x16x32_bf16 v[108:111], v[72:75], v[180:183], v[108:111]\n\t"
    "ds_read_b64_tr_b16 v[58:59], v15 offset:2048\n\t"
    "v_mfma_f32_16x16x32_bf16 v[108:111], v[76:79], v[188:191], v[108:111]\n\t"
    "ds_read_b64_tr_b16 v[60:61], v15 offset:4096\n\t"
    "s_waitcnt lgkmcnt(3)\n\t"
    "v_mfma_f32_16x16x32_bf16 v[80:83], v[48:51], v[192:195], v[80:83]\n\t"
    "ds_read_b64_tr_b16 v[62:63], v15 offset:6144\n\t"
    "global_load_dwordx4 v[128:131], v3, s[62:63] offset:-3072 sc1\n\t"
    "v_mfma_f32_16x16x32_bf16 v[80:83], v[52:55], v[200:203], v[80:83]\n\t"
    "ds_read_b64_tr_b16 v[64:65], v17 offset:16384\n\t"
    "v_mfma_f32_16x16x32_bf16 v[84:87], v[48:51], v[196:199], v[84:87]\n\t"
    "ds_read_b64_tr_b16 v[66:67], v17 offset:18432\n\t"
    "v_mfma_f32_16x16x32_bf16 v[84:87], v[52:55], v[204:207], v[84:87]\n\t"
    "ds_read_b64_tr_b16 v[68:69], v17 offset:20480\n\t"
    "s_waitcnt lgkmcnt(3)\n\t"
    "v_mfma_f32_16x16x32_bf16 v[88:91], v[56:59], v[192:195], v[88:91]\n\t"
    "ds_read_b64_tr_b16 v[70:71], v17 offset:22528\n\t"
    "global_load_dwordx4 v[132:135], v3, s[66:67] offset:-3072 sc1\n\t"
    "v_mfma_f32_16x16x32_bf16 v[88:91], v[60:63], v[200:203], v[88:91]\n\t"
    "ds_read_b64_tr_b16 v[72:73], v18 offset:0\n\t"
    "v_mfma_f32_16x16x32_bf16 v[92:95], v[56:59], v[196:199], v[92:95]\n\t"
    "ds_read_b64_tr_b16 v[74:75], v18 offset:2048\n\t"
    "v_mfma_f32_16x16x32_bf16 v[92:95], v[60:63], v[204:207], v[92:95]\n\t"
    "ds_read_b64_tr_b16 v[76:77], v18 offset:4096\n\t"
    "s_waitcnt lgkmcnt(3)\n\t"
    "v_mfma_f32_16x16x32_bf16 v[96:99], v[64:67], v[192:195], v[96:99]\n\t"
    "ds_read_b64_tr_b16 v[78:79], v18 offset:6144\n\t"
    "v_mfma_f32_16x16x32_bf16 v[96:99], v[68:71], v[200:203], v[96:99]\n\t"
    "ds_read_b64_tr_b16 v[48:49], v14 offset:24576\n\t"
    "s_waitcnt vmcnt(10)\n\t"
    "s_add_u32 s89, s74, 131072\n\t"
    "s_mov_b64 exec, 1\n\t"
    "buffer_atomic_swap_x2 v[32:33], v0, %[sw], s89 offen\n\t"
    "s_mov_b64 exec, -1\n\t"
    "v_mfma_f32_16x16x32_bf16 v[100:103], v[64:67], v[196:199], v[100:103]\n\t"
    "ds_read_b64_tr_b16 v[50:51], v14 offset:26624\n\t"
    "global_load_dwordx4 v[136:139], v3, s[96:97] offset:-3072 sc1\n\t"
    "v_mfma_f32_16x16x32_bf16 v[100:103], v[68:71], v[204:207], v[100:103]\n\t"
    "ds_read_b64_tr_b16 v[52:53], v14 offset:28672\n\t"
    "s_waitcnt lgkmcnt(3)\n\t"
    "v_mfma_f32_16x16x32_bf16 v[104:107], v[72:75], v[192:195], v[104:107]\n\t"
    "ds_read_b64_tr_b16 v[54:55], v14 offset:30720\n\t"
    "v_mfma_f32_16x16x32_bf16 v[104:107], v[76:79], v[200:203], v[104:107]\n\t"
    "ds_read_b64_tr_b16 v[56:57], v15 offset:8192\n\t"
    "v_mfma_f32_16x16x32_bf16 v[108:111], v[72:75], v[196:199], v[108:111]\n\t"
    "ds_read_b64_tr_b16 v[58:59], v15 offset:10240\n\t"
    "global_load_dwordx4 v[140:143], v3, s[98:99] offset:-3072 sc1\n\t"
    "v_mfma_f32_16x16x32_bf16 v[108:111], v[76:79], v[204:207], v[108:111]\n\t"
    "ds_read_b64_tr_b16 v[60:61], v15 offset:12288\n\t"
    "s_waitcnt lgkmcnt(3)\n\t"
    "v_mfma_f32_16x16x32_bf16 v[80:83], v[48:51], v[208:211], v[80:83]\n\t"
    "ds_read_b64_tr_b16 v[62:63], v15 offset:14336\n\t"
    "v_mfma_f32_16x16x32_bf16 v[80:83], v[52:55], v[216:219], v[80:83]\n\t"
    "ds_read_b64_tr_b16 v[64:65], v17 offset:24576\n\t"
    "v_mfma_f32_16x16x32_bf16 v[84:87], v[48:51], v[212:215], v[84:87]\n\t"
    "ds_read_b64_tr_b16 v[66:67], v17 offset:26624\n\t"
    "global_load_dwordx4 v[144:147], v3, s[62:63] offset:0 sc1\n\t"
    "v_mfma_f32_16x16x32_bf16 v[84:87], v[52:55], v[220:223], v[84:87]\n\t"
    "ds_read_b64_tr_b16 v[68:69], v17 offset:28672\n\t"
    "s_waitcnt lgkmcnt(3)\n\t"
    "v_mfma_f32_16x16x32_bf16 v[88:91], v[56:59], v[208:211], v[88:91]\n\t"
    "ds_read_b64_tr_b16 v[70:71], v17 offset:30720\n\t"
    "v_mfma_f32_16x16x32_bf16 v[88:91], v[60:63], v[216:219], v[88:91]\n\t"
    "ds_read_b64_tr_b16 v[72:73], v18 offset:8192\n\t"
    "v_mfma_f32_16x16x32_bf16 v[92:95], v[56:59], v[212:215], v[92:95]\n\t"
    "ds_read_b64_tr_b16 v[74:75], v18 offset:10240\n\t"
    "global_load_dwordx4 v[148:151], v3, s[66:67] offset:0 sc1\n\t"
    "v_mfma_f32_16x16x32_bf16 v[92:95], v[60:63], v[220:223], v[92:95]\n\t"
    "ds_read_b64_tr_b16 v[76:77], v18 offset:12288\n\t"
    "s_waitcnt lgkmcnt(3)\n\t"
    "v_mfma_f32_16x16x32_bf16 v[96:99], v[64:67], v[208:211], v[96:99]\n\t"
    "ds_read_b64_tr_b16 v[78:79], v18 offset:14336\n\t"
    "v_mfma_f32_16x16x32_bf16 v[96:99], v[68:71], v[216:219], v[96:99]\n\t"
    "ds_read_b64_tr_b16 v[48:49], v14 offset:32768\n\t"
    "v_mfma_f32_16x16x32_bf16 v[100:103], v[64:67], v[212:215], v[100:103]\n\t"
    "ds_read_b64_tr_b16 v[50:51], v14 offset:34816\n\t"
    "global_load_dwordx4 v[152:155], v3, s[96:97] offset:0 sc1\n\t"
    "v_mfma_f32_16x16x32_bf16 v[100:103], v[68:71], v[220:223], v[100:103]\n\t"
    "ds_read_b64_tr_b16 v[52:53], v14 offset:36864\n\t"
    "s_waitcnt lgkmcnt(3)\n\t"
    "v_mfma_f32_16x16x32_bf16 v[104:107], v[72:75], v[208:211], v[104:107]\n\t"
    "ds_read_b64_tr_b16 v[54:55], v14 offset:38912\n\t"
    "v_mfma_f32_16x16x32_bf16 v[104:107], v[76:79], v[216:219], v[104:107]\n\t"
    "ds_read_b64_tr_b16 v[56:57], v15 offset:16384\n\t"
    "v_mfma_f32_16x16x32_bf16 v[108:111], v[72:75], v[212:215], v[108:111]\n\t"
    "ds_read_b64_tr_b16 v[58:59], v15 offset:18432\n\t"
    "global_load_dwordx4 v[156:159], v3, s[98:99] offset:0 sc1\n\t"
    "v_mfma_f32_16x16x32_bf16 v[108:111], v[76:79], v[220:223], v[108:111]\n\t"
    "ds_read_b64_tr_b16 v[60:61], v15 offset:20480\n\t"
    "s_waitcnt lgkmcnt(3)\n\t"
    "v_mfma_f32_16x16x32_bf16 v[80:83], v[48:51], a[224:227], v[80:83]\n\t"
    "ds_read_b64_tr_b16 v[62:63], v15 offset:22528\n\t"
    "v_mfma_f32_16x16x32_bf16 v[80:83], v[52:55], a[232:235], v[80:83]\n\t"
    "ds_read_b64_tr_b16 v[64:65], v17 offset:32768\n\t"
    "v_mfma_f32_16x16x32_bf16 v[84:87], v[48:51], a[228:231], v[84:87]\n\t"
    "ds_read_b64_tr_b16 v[66:67], v17 offset:34816\n\t"
    "v_mfma_f32_16x16x32_bf16 v[84:87], v[52:55], a[236:239], v[84:87]\n\t"
    "ds_read_b64_tr_b16 v[68:69], v17 offset:36864\n\t"
    "s_waitcnt lgkmcnt(3)\n\t"
    "v_mfma_f32_16x16x32_bf16 v[88:91], v[56:59], a[224:227], v[88:91]\n\t"
    "ds_read_b64_tr_b16 v[70:71], v17 offset:38912\n\t"
    "global_load_dwordx4 v[160:163], v40, s[42:43] offset:0 sc1\n\t"
    "v_mfma_f32_16x16x32_bf16 v[88:91], v[60:63], a[232:235], v[88:91]\n\t"
    "ds_read_b64_tr_b16 v[72:73], v18 offset:16384\n\t"
    "v_mfma_f32_16x16x32_bf16 v[92:95], v[56:59], a[228:231], v[92:95]\n\t"
    "ds_read_b64_tr_b16 v[74:75], v18 offset:18432\n\t"
    "v_mfma_f32_16x16x32_bf16 v[92:95], v[60:63], a[236:239], v[92:95]\n\t"
    "ds_read_b64_tr_b16 v[76:77], v18 offset:20480\n\t"
    "s_waitcnt lgkmcnt(3)\n\t"
    "v_mfma_f32_16x16x32_bf16 v[96:99], v[64:67], a[224:227], v[96:99]\n\t"
    "ds_read_b64_tr_b16 v[78:79], v18 offset:22528\n\t"
    "global_load_dwordx4 v[164:167], v4, s[42:43] offset:0 sc1\n\t"
    "v_mfma_f32_16x16x32_bf16 v[96:99], v[68:71], a[232:235], v[96:99]\n\t"
    "ds_read_b64_tr_b16 v[48:49], v14 offset:40960\n\t"
    "v_mfma_f32_16x16x32_bf16 v[100:103], v[64:67], a[228:231], v[100:103]\n\t"
    "ds_read_b64_tr_b16 v[50:51], v14 offset:43008\n\t"
    "v_mfma_f32_16x16x32_bf16 v[100:103], v[68:71], a[236:239], v[100:103]\n\t"
    "ds_read_b64_tr_b16 v[52:53], v14 offset:45056\n\t"
    "s_waitcnt lgkmcnt(3)\n\t"
    "v_mfma_f32_16x16x32_bf16 v[104:107], v[72:75], a[224:227], v[104:107]\n\t"
    "ds_read_b64_tr_b16 v[54:55], v14 offset:47104\n\t"
    "global_load_dwordx4 v[168:171], v40, s[44:45] offset:0 sc1\n\t"
    "v_mfma_f32_16x16x32_bf16 v[104:107], v[76:79], a[232:235], v[104:107]\n\t"
    "ds_read_b64_tr_b16 v[56:57], v15 offset:24576\n\t"
    "v_mfma_f32_16x16x32_bf16 v[108:111], v[72:75], a[228:231], v[108:111]\n\t"
    "ds_read_b64_tr_b16 v[58:59], v15 offset:26624\n\t"
    "v_mfma_f32_16x16x32_bf16 v[108:111], v[76:79], a[236:239], v[108:111]\n\t"
    "ds_read_b64_tr_b16 v[60:61], v15 offset:28672\n\t"
    "s_waitcnt lgkmcnt(3)\n\t"
    "v_mfma_f32_16x16x32_bf16 v[80:83], v[48:51], a[240:243], v[80:83]\n\t"
    "ds_read_b64_tr_b16 v[62:63], v15 offset:30720\n\t"
    "global_load_dwordx4 v[172:175], v4, s[44:45] offset:0 sc1\n\t"
    "v_mfma_f32_16x16x32_bf16 v[80:83], v[52:55], a[248:251], v[80:83]\n\t"
    "ds_read_b64_tr_b16 v[64:65], v17 offset:40960\n\t"
    "v_mfma_f32_16x16x32_bf16 v[84:87], v[48:51], a[244:247], v[84:87]\n\t"
    "ds_read_b64_tr_b16 v[66:67], v17 offset:43008\n\t"
    "v_mfma_f32_16x16x32_bf16 v[84:87], v[52:55], a[252:255], v[84:87]\n\t"
    "ds_read_b64_tr_b16 v[68:69], v17 offset:45056\n\t"
    "s_add_u32 s89, s75, 131072\n\t"
    "v_mov_b64 v[30:31], s[56:57]\n\t"
    "s_mov_b64 exec, 0xff\n\t"
    "v_mov_b64 v[30:31], 0\n\t"
    "buffer_atomic_or_x2 v[30:31], v20, %[sw], s89 offen sc0\n\t"
    "s_mov_b64 exec, -1\n\t"
    "s_waitcnt lgkmcnt(3)\n\t"
    "v_mfma_f32_16x16x32_bf16 v[88:91], v[56:59], a[240:243], v[88:91]\n\t"
    "ds_read_b64_tr_b16 v[70:71], v17 offset:47104\n\t"
    "v_mfma_f32_16x16x32_bf16 v[88:91], v[60:63], a[248:251], v[88:91]\n\t"
    "ds_read_b64_tr_b16 v[72:73], v18 offset:24576\n\t"
    "global_load_dwordx4 v[176:179], v40, s[42:43] offset:2048 sc1\n\t"
    "v_mfma_f32_16x16x32_bf16 v[92:95], v[56:59], a[244:247], v[92:95]\n\t"
    "ds_read_b64_tr_b16 v[74:75], v18 offset:26624\n\t"
    "v_mfma_f32_16x16x32_bf16 v[92:95], v[60:63], a[252:255], v[92:95]\n\t"
    "ds_read_b64_tr_b16 v[76:77], v18 offset:28672\n\t"
    "s_waitcnt lgkmcnt(3)\n\t"
    "v_mfma_f32_16x16x32_bf16 v[96:99], v[64:67], a[240:243], v[96:99]\n\t"
    "ds_read_b64_tr_b16 v[78:79], v18 offset:30720\n\t"
    "v_mfma_f32_16x16x32_bf16 v[96:99], v[68:71], a[248:251], v[96:99]\n\t"
    "global_load_dwordx4 v[180:183], v4, s[42:43] offset:2048 sc1\n\t"
    "v_mfma_f32_16x16x32_bf16 v[100:103], v[64:67], a[244:247], v[100:103]\n\t"
    "v_mfma_f32_16x16x32_bf16 v[100:103], v[68:71], a[252:255], v[100:103]\n\t"
    "s_waitcnt lgkmcnt(0)\n\t"
    "v_mfma_f32_16x16x32_bf16 v[104:107], v[72:75], a[240:243], v[104:107]\n\t"
    "v_mfma_f32_16x16x32_bf16 v[104:107], v[76:79], a[248:251], v[104:107]\n\t"
    "global_load_dwordx4 v[184:187], v40, s[44:45] offset:2048 sc1\n\t"
    "v_mfma_f32_16x16x32_bf16 v[108:111], v[72:75], a[244:247], v[108:111]\n\t"
    "v_mfma_f32_16x16x32_bf16 v[108:111], v[76:79], a[252:255], v[108:111]\n\t"
    "s_waitcnt lgkmcnt(0)\n\t"
    "s_barrier\n\t"
    "ds_write_b32 v41, v34\n\t"
    "s_waitcnt vmcnt(21)\n\t"
    "v_mfma_f32_16x16x32_bf16 v[228:231], a[0:3], a[196:199], 0\n\t"
    "v_mfma_f32_16x16x32_bf16 v[244:247], a[4:7], a[196:199], 0\n\t"
    "s_add_u32 s68, s86, 425984\n\t"
    "s_addc_u32 s69, s87, 0\n\t"
    "v_mul_f32 v46, v80, v88\n\t"
    "v_mul_f32 v47, v81, v89\n\t"
    "v_mfma_f32_16x16x32_bf16 v[116:119], a[8:11], a[196:199], 0\n\t"
    "s_sub_u32 m0, s81, 32768\n\t"
    "v_cvt_pk_bf16_f32 v24, v46, v47\n\t"
    "v_mul_f32 v46, v82, v90\n\t"
    "v_mfma_f32_16x16x32_bf16 v[52:55], a[12:15], a[196:199], 0\n\t"
    "global_load_lds_dwordx4 v2, s[68:69] offset:0\n\t"
    "v_mul_f32 v47, v83, v91\n\t"
    "v_cvt_pk_bf16_f32 v25, v46, v47\n\t"
    "v_mfma_f32_16x16x32_bf16 v[232:235], a[0:3], a[200:203], 0\n\t"
    "v_mul_f32 v46, v96, v104\n\t"
    "v_mul_f32 v47, v97, v105\n\t"
    "v_cvt_pk_bf16_f32 v26, v46, v47\n\t"
    "v_mfma_f32_16x16x32_bf16 v[248:251], a[4:7], a[200:203], 0\n\t"
    "global_load_lds_dwordx4 v19, s[68:69] offset:1024\n\t"
    "v_mul_f32 v46, v98, v106\n\t"
    "v_mul_f32 v47, v99, v107\n\t"
    "v_mfma_f32_16x16x32_bf16 v[120:123], a[8:11], a[200:203], 0\n\t"
    "v_cvt_pk_bf16_f32 v27, v46, v47\n\t"
    "global_store_dwordx4 v0, v[24:27], s[90:91] offset:2048 sc1\n\t"
    "v_mul_f32 v46, v84, v92\n\t"
    "v_mfma_f32_16x16x32_bf16 v[56:59], a[12:15], a[200:203], 0\n\t"
    "v_mul_f32 v47, v85, v93\n\t"
    "v_cvt_pk_bf16_f32 v36, v46, v47\n\t"
    "v_mul_f32 v46, v86, v94\n\t"
    "v_mfma_f32_16x16x32_bf16 v[236:239], a[0:3], a[204:207], 0\n\t"
    "v_mul_f32 v47, v87, v95\n\t"
    "v_cvt_pk_bf16_f32 v37, v46, v47\n\t"
    "v_mul_f32 v46, v100, v108\n\t"
    "v_mfma_f32_16x16x32_bf16 v[252:255], a[4:7], a[204:207], 0\n\t"
    "v_mul_f32 v47, v101, v109\n\t"
    "v_cvt_pk_bf16_f32 v38, v46, v47\n\t"
    "v_mul_f32 v46, v102, v110\n\t"
    "v_mfma_f32_16x16x32_bf16 v[124:127], a[8:11], a[204:207], 0\n\t"
    "v_mul_f32 v47, v103, v111\n\t"
    "v_cvt_pk_bf16_f32 v39, v46, v47\n\t"
    "global_store_dwordx4 v0, v[36:39], s[94:95] offset:2048 sc1\n\t"
    "v_mfma_f32_16x16x32_bf16 v[60:63], a[12:15], a[204:207], 0\n\t"
    "v_mfma_f32_16x16x32_bf16 v[224:227], a[0:3], a[192:195], 0\n\t"
    "v_mfma_f32_16x16x32_bf16 v[240:243], a[4:7], a[192:195], 0\n\t"
    "v_mfma_f32_16x16x32_bf16 v[112:115], a[8:11], a[192:195], 0\n\t"
    "v_mfma_f32_16x16x32_bf16 v[48:51], a[12:15], a[192:195], 0\n\t"
    "s_waitcnt vmcnt(21)\n\t"
    "v_mfma_f32_16x16x32_bf16 v[228:231], a[16:19], a[212:215], v[228:231]\n\t"
    "v_mfma_f32_16x16x32_bf16 v[244:247], a[20:23], a[212:215], v[244:247]\n\t"
    "v_mfma_f32_16x16x32_bf16 v[116:119], a[24:27], a[212:215], v[116:119]\n\t"
    "s_add_u32 s68, s86, 440320\n\t"
    "s_addc_u32 s69, s87, 0\n\t"
    "v_mfma_f32_16x16x32_bf16 v[52:55], a[28:31], a[212:215], v[52:55]\n\t"
    "global_load_lds_dwordx4 v2, s[68:69] offset:2048\n\t"
    "v_mfma_f32_16x16x32_bf16 v[232:235], a[16:19], a[216:219], v[232:235]\n\t"
    "v_mfma_f32_16x16x32_bf16 v[248:251], a[20:23], a[216:219], v[248:251]\n\t"
    "v_mfma_f32_16x16x32_bf16 v[120:123], a[24:27], a[216:219], v[120:123]\n\t"
    "global_load_lds_dwordx4 v19, s[68:69] offset:3072\n\t"
    "v_mfma_f32_16x16x32_bf16 v[56:59], a[28:31], a[216:219], v[56:59]\n\t"
    "v_mfma_f32_16x16x32_bf16 v[236:239], a[16:19], a[220:223], v[236:239]\n\t"
    "v_mfma_f32_16x16x32_bf16 v[252:255], a[20:23], a[220:223], v[252:255]\n\t"
    "v_mfma_f32_16x16x32_bf16 v[124:127], a[24:27], a[220:223], v[124:127]\n\t"
    "global_load_dwordx4 v[188:191], v4, s[44:45] offset:2048 sc1\n\t"
    "v_mfma_f32_16x16x32_bf16 v[60:63], a[28:31], a[220:223], v[60:63]\n\t"
    "s_waitcnt vmcnt(10)\n\t"
    "v_cmp_ne_u64 vcc, v[30:31], s[56:57]\n\t"
    "s_cmp_eq_u64 vcc, 0\n\t"
    "s_cbranch_scc1 101f\n\t"
    "s_mov_b32 s88, 0x40000\n\t"
    "100:\n\t"
    "s_add_u32 s89, s75, 131072\n\t"
    "v_mov_b64 v[30:31], s[56:57]\n\t"
    "s_mov_b64 exec, 0xff\n\t"
    "v_mov_b64 v[30:31], 0\n\t"
    "buffer_atomic_or_x2 v[30:31], v20, %[sw], s89 offen sc0\n\t"
    "s_mov_b64 exec, -1\n\t"
    "s_waitcnt vmcnt(0)\n\t"
    "v_cmp_ne_u64 vcc, v[30:31], s[56:57]\n\t"
    "s_cmp_eq_u64 vcc, 0\n\t"
    "s_cbranch_scc1 101f\n\t"
    "s_sub_u32 s88, s88, 1\n\t"
    "s_cmp_lg_u32 s88, 0\n\t"
    "s_cbranch_scc1 100b\n\t"
    "101:\n\t"
    "v_mfma_f32_16x16x32_bf16 v[224:227], a[16:19], a[208:211], v[224:227]\n\t"
    "v_mfma_f32_16x16x32_bf16 v[240:243], a[20:23], a[208:211], v[240:243]\n\t"
    "v_mfma_f32_16x16x32_bf16 v[112:115], a[24:27], a[208:211], v[112:115]\n\t"
    "v_mfma_f32_16x16x32_bf16 v[48:51], a[28:31], a[208:211], v[48:51]\n\t"
    "global_load_dwordx4 v[192:195], v0, s[62:63] offset:-2048 sc1\n\t"
    "v_mfma_f32_16x16x32_bf16 v[228:231], a[32:35], v[132:135], v[228:231]\n\t"
    "v_mfma_f32_16x16x32_bf16 v[228:231], a[48:51], v[148:151], v[228:231]\n\t"
    "v_mfma_f32_16x16x32_bf16 v[244:247], a[36:39], v[132:135], v[244:247]\n\t"
    "v_mfma_f32_16x16x32_bf16 v[244:247], a[52:55], v[148:151], v[244:247]\n\t"
    "global_load_dwordx4 v[196:199], v0, s[66:67] offset:-2048 sc1\n\t"
    "v_mfma_f32_16x16x32_bf16 v[116:119], a[40:43], v[132:135], v[116:119]\n\t"
    "v_mfma_f32_16x16x32_bf16 v[116:119], a[56:59], v[148:151], v[116:119]\n\t"
    "v_mfma_f32_16x16x32_bf16 v[52:55], a[44:47], v[132:135], v[52:55]\n\t"
    "v_mfma_f32_16x16x32_bf16 v[52:55], a[60:63], v[148:151], v[52:55]\n\t"
    "global_load_dwordx4 v[200:203], v0, s[96:97] offset:-2048 sc1\n\t"
    "ds_read_b64 v[88:89], v23 offset:0\n\t"
    "ds_read_b64 v[90:91], v23 offset:32\n\t"
    "v_mfma_f32_16x16x32_bf16 v[232:235], a[32:35], v[136:139], v[232:235]\n\t"
    "ds_read_b64 v[92:93], v23 offset:64\n\t"
    "ds_read_b64 v[94:95], v23 offset:96\n\t"
    "v_add_u32 v96, s78, v0\n\t"
    "v_mfma_f32_16x16x32_bf16 v[232:235], a[48:51], v[152:155], v[232:235]\n\t"
    "v_add_u32 v97, s79, v0\n\t"
    "v_add_u32 v98, s80, v0\n\t"
    "s_lshl_b32 s84, s82, 2\n\t"
    "v_mfma_f32_16x16x32_bf16 v[248:251], a[36:39], v[136:139], v[248:251]\n\t"
    "v_add_u32 v99, s84, v0\n\t"
    "ds_read_b64_tr_b16 a[176:177], v42 offset:4096\n\t"
    "v_mfma_f32_16x16x32_bf16 v[248:251], a[52:55], v[152:155], v[248:251]\n\t"
    "global_load_dwordx4 v[204:207], v0, s[98:99] offset:-2048 sc1\n\t"
    "ds_read_b64_tr_b16 a[178:179], v42 offset:6144\n\t"
    "ds_read_b64_tr_b16 a[180:181], v43 offset:4096\n\t"
    "v_mfma_f32_16x16x32_bf16 v[120:123], a[40:43], v[136:139], v[120:123]\n\t"
    "ds_read_b64_tr_b16 a[182:183], v43 offset:6144\n\t"
    "ds_read_b64_tr_b16 a[184:185], v44 offset:4096\n\t"
    "ds_read_b64_tr_b16 a[186:187], v44 offset:6144\n\t"
    "v_mfma_f32_16x16x32_bf16 v[120:123], a[56:59], v[152:155], v[120:123]\n\t"
    "ds_read_b64_tr_b16 a[188:189], v45 offset:4096\n\t"
    "ds_read_b64_tr_b16 a[190:191], v45 offset:6144\n\t"
    "v_mfma_f32_16x16x32_bf16 v[56:59], a[44:47], v[136:139], v[56:59]\n\t"
    "ds_read_b64_tr_b16 a[192:193], v42 offset:0\n\t"
    "ds_read_b64_tr_b16 a[194:195], v42 offset:2048\n\t"
    "s_waitcnt lgkmcnt(10)\n\t"
    "ds_read_b64_tr_b16 a[196:197], v43 offset:0\n\t"
    "v_mfma_f32_16x16x32_bf16 v[56:59], a[60:63], v[152:155], v[56:59]\n\t"
    "s_waitcnt vmcnt(7)\n\t"
    "s_add_u32 s89, s74, 262144\n\t"
    "s_mov_b64 exec, 1\n\t"
    "buffer_atomic_swap_x2 v[32:33], v0, %[sw], s89 offen\n\t"
    "s_mov_b64 exec, -1\n\t"
    "v_mfma_f32_16x16x32_bf16 v[236:239], a[32:35], v[140:143], v[236:239]\n\t"
    "global_load_dwordx4 v[208:211], v0, s[62:63] offset:1024 sc1\n\t"
    "ds_read_b64_tr_b16 a[198:199], v43 offset:2048\n\t"
    "ds_read_b64_tr_b16 a[200:201], v44 offset:0\n\t"
    "v_mfma_f32_16x16x32_bf16 v[236:239], a[48:51], v[156:159], v[236:239]\n\t"
    "ds_read_b64_tr_b16 a[202:203], v44 offset:2048\n\t"
    "ds_read_b64_tr_b16 a[204:205], v45 offset:0\n\t"
    "s_waitcnt lgkmcnt(0)\n\t"
    "ds_read_b64_tr_b16 a[206:207], v45 offset:2048\n\t"
    "v_mfma_f32_16x16x32_bf16 v[252:255], a[36:39], v[140:143], v[252:255]\n\t"
    "v_subrev_u32 v42, 0x8000, v42\n\t"
    "v_subrev_u32 v43, 0x8000, v43\n\t"
    "v_subrev_u32 v44, 0x8000, v44\n\t"
    "v_mfma_f32_16x16x32_bf16 v[252:255], a[52:55], v[156:159], v[252:255]\n\t"
    "v_subrev_u32 v45, 0x8000, v45\n\t"
    "v_mfma_f32_16x16x32_bf16 v[124:127], a[40:43], v[140:143], v[124:127]\n\t"
    "global_load_dwordx4 v[212:215], v0, s[66:67] offset:1024 sc1\n\t"
    "v_mfma_f32_16x16x32_bf16 v[124:127], a[56:59], v[156:159], v[124:127]\n\t"
    "v_mfma_f32_16x16x32_bf16 v[60:63], a[44:47], v[140:143], v[60:63]\n\t"
    "v_mfma_f32_16x16x32_bf16 v[60:63], a[60:63], v[156:159], v[60:63]\n\t"
    "v_mfma_f32_16x16x32_bf16 v[224:227], a[32:35], v[128:131], v[224:227]\n\t"
    "global_load_dwordx4 v[216:219], v0, s[96:97] offset:1024 sc1\n\t"
    "v_mfma_f32_16x16x32_bf16 v[224:227], a[48:51], v[144:147], v[224:227]\n\t"
    "v_mfma_f32_16x16x32_bf16 v[240:243], a[36:39], v[128:131], v[240:243]\n\t"
    "v_mfma_f32_16x16x32_bf16 v[240:243], a[52:55], v[144:147], v[240:243]\n\t"
    "v_mfma_f32_16x16x32_bf16 v[112:115], a[40:43], v[128:131], v[112:115]\n\t"
    "global_load_dwordx4 v[220:223], v0, s[98:99] offset:1024 sc1\n\t"
    "v_mfma_f32_16x16x32_bf16 v[112:115], a[56:59], v[144:147], v[112:115]\n\t"
    "v_mfma_f32_16x16x32_bf16 v[48:51], a[44:47], v[128:131], v[48:51]\n\t"
    "v_mfma_f32_16x16x32_bf16 v[48:51], a[60:63], v[144:147], v[48:51]\n\t"
    "s_waitcnt vmcnt(9)\n\t"
    "s_bitcmp1_b32 s60, 0\n\t"
    "s_cbranch_scc1 112f\n\t"
    "v_swap_b32 v162, v164\n\t"
    "v_swap_b32 v163, v165\n\t"
    "v_swap_b32 v170, v172\n\t"
    "v_swap_b32 v171, v173\n\t"
    "s_branch 113f\n\t"
    "112:\n\t"
    "v_swap_b32 v160, v166\n\t"
    "v_swap_b32 v161, v167\n\t"
    "v_swap_b32 v168, v174\n\t"
    "v_swap_b32 v169, v175\n\t"
    "113:\n\t"
    "s_nop 1\n\t"
    "s_bitcmp1_b32 s60, 0\n\t"
    "s_cbranch_scc1 122f\n\t"
    "v_swap_b32 v178, v180\n\t"
    "v_swap_b32 v179, v181\n\t"
    "v_swap_b32 v186, v188\n\t"
    "v_swap_b32 v187, v189\n\t"
    "s_branch 123f\n\t"
    "122:\n\t"
    "v_swap_b32 v176, v182\n\t"
    "v_swap_b32 v177, v183\n\t"
    "v_swap_b32 v184, v190\n\t"
    "v_swap_b32 v185, v191\n\t"
    "123:\n\t"
    "s_nop 1\n\t"
    "v_mfma_f32_16x16x32_bf16 v[228:231], a[64:67], v[164:167], v[228:231]\n\t"
    "v_mfma_f32_16x16x32_bf16 v[228:231], a[80:83], v[180:183], v[228:231]\n\t"
    "global_load_dwordx4 a[224:227], v3, s[62:63] offset:-2048 sc1\n\t"
    "v_mfma_f32_16x16x32_bf16 v[244:247], a[68:71], v[164:167], v[244:247]\n\t"
    "v_mfma_f32_16x16x32_bf16 v[244:247], a[84:87], v[180:183], v[244:247]\n\t"
    "v_mfma_f32_16x16x32_bf16 v[116:119], a[72:75], v[164:167], v[116:119]\n\t"
    "v_mfma_f32_16x16x32_bf16 v[116:119], a[88:91], v[180:183], v[116:119]\n\t"
    "s_add_u32 s89, s75, 262144\n\t"
    "v_mov_b64 v[30:31], s[56:57]\n\t"
    "s_mov_b64 exec, 0xff\n\t"
    "v_mov_b64 v[30:31], 0\n\t"
    "buffer_atomic_or_x2 v[30:31], v20, %[sw], s89 offen sc0\n\t"
    "s_mov_b64 exec, -1\n\t"
    "v_mfma_f32_16x16x32_bf16 v[52:55], a[76:79], v[164:167], v[52:55]\n\t"
    "global_load_dwordx4 a[228:231], v3, s[66:67] offset:-2048 sc1\n\t"
    "v_mfma_f32_16x16x32_bf16 v[52:55], a[92:95], v[180:183], v[52:55]\n\t"
    "v_mfma_f32_16x16x32_bf16 v[232:235], a[64:67], v[168:171], v[232:235]\n\t"
    "v_mfma_f32_16x16x32_bf16 v[232:235], a[80:83], v[184:187], v[232:235]\n\t"
    "v_mfma_f32_16x16x32_bf16 v[248:251], a[68:71], v[168:171], v[248:251]\n\t"
    "global_load_dwordx4 a[232:235], v3, s[96:97] offset:-2048 sc1\n\t"
    "ds_read_b64_tr_b16 a[208:209], v42 offset:0\n\t"
    "v_mfma_f32_16x16x32_bf16 v[248:251], a[84:87], v[184:187], v[248:251]\n\t"
    "ds_read_b64_tr_b16 a[210:211], v42 offset:2048\n\t"
    "ds_read_b64_tr_b16 a[212:213], v43 offset:0\n\t"
    "ds_read_b64_tr_b16 a[214:215], v43 offset:2048\n\t"
    "v_mfma_f32_16x16x32_bf16 v[120:123], a[72:75], v[168:171], v[120:123]\n\t"
    "ds_read_b64_tr_b16 a[216:217], v44 offset:0\n\t"
    "ds_read_b64_tr_b16 a[218:219], v44 offset:2048\n\t"
    "ds_read_b64_tr_b16 a[220:221], v45 offset:0\n\t"
    "v_mfma_f32_16x16x32_bf16 v[120:123], a[88:91], v[184:187], v[120:123]\n\t"
    "ds_read_b64_tr_b16 a[222:223], v45 offset:2048\n\t"
    "v_mfma_f32_16x16x32_bf16 v[56:59], a[76:79], v[168:171], v[56:59]\n\t"
    "global_load_dwordx4 a[236:239], v3, s[98:99] offset:-2048 sc1\n\t"
    "v_mfma_f32_16x16x32_bf16 v[56:59], a[92:95], v[184:187], v[56:59]\n\t"
    "v_mfma_f32_16x16x32_bf16 v[236:239], a[64:67], v[172:175], v[236:239]\n\t"
    "v_mfma_f32_16x16x32_bf16 v[236:239], a[80:83], v[188:191], v[236:239]\n\t"
    "v_mfma_f32_16x16x32_bf16 v[252:255], a[68:71], v[172:175], v[252:255]\n\t"
    "v_mfma_f32_16x16x32_bf16 v[252:255], a[84:87], v[188:191], v[252:255]\n\t"
    "global_load_dwordx4 a[240:243], v3, s[62:63] offset:1024 sc1\n\t"
    "v_mfma_f32_16x16x32_bf16 v[124:127], a[72:75], v[172:175], v[124:127]\n\t"
    "v_mfma_f32_16x16x32_bf16 v[124:127], a[88:91], v[188:191], v[124:127]\n\t"
    "v_mfma_f32_16x16x32_bf16 v[60:63], a[76:79], v[172:175], v[60:63]\n\t"
    "v_mfma_f32_16x16x32_bf16 v[60:63], a[92:95], v[188:191], v[60:63]\n\t"
    "global_load_dwordx4 a[244:247], v3, s[66:67] offset:1024 sc1\n\t"
    "v_mfma_f32_16x16x32_bf16 v[224:227], a[64:67], v[160:163], v[224:227]\n\t"
    "v_mfma_f32_16x16x32_bf16 v[224:227], a[80:83], v[176:179], v[224:227]\n\t"
    "v_mfma_f32_16x16x32_bf16 v[240:243], a[68:71], v[160:163], v[240:243]\n\t"
    "v_mfma_f32_16x16x32_bf16 v[240:243], a[84:87], v[176:179], v[240:243]\n\t"
    "global_load_dwordx4 a[248:251], v3, s[96:97] offset:1024 sc1\n\t"
    "v_mfma_f32_16x16x32_bf16 v[112:115], a[72:75], v[160:163], v[112:115]\n\t"
    "v_mfma_f32_16x16x32_bf16 v[112:115], a[88:91], v[176:179], v[112:115]\n\t"
    "v_mfma_f32_16x16x32_bf16 v[48:51], a[76:79], v[160:163], v[48:51]\n\t"
    "v_mfma_f32_16x16x32_bf16 v[48:51], a[92:95], v[176:179], v[48:51]\n\t"
    "global_load_dwordx4 a[252:255], v3, s[98:99] offset:1024 sc1\n\t"
    "s_waitcnt vmcnt(9)\n\t"
    "v_mfma_f32_16x16x32_bf16 v[228:231], a[96:99], v[196:199], v[228:231]\n\t"
    "v_mfma_f32_16x16x32_bf16 v[228:231], a[112:115], v[212:215], v[228:231]\n\t"
    "v_mfma_f32_16x16x32_bf16 v[244:247], a[100:103], v[196:199], v[244:247]\n\t"
    "v_mfma_f32_16x16x32_bf16 v[244:247], a[116:119], v[212:215], v[244:247]\n\t"
    "v_mfma_f32_16x16x32_bf16 v[116:119], a[104:107], v[196:199], v[116:119]\n\t"
    "v_mfma_f32_16x16x32_bf16 v[116:119], a[120:123], v[212:215], v[116:119]\n\t"
    "v_mfma_f32_16x16x32_bf16 v[52:55], a[108:111], v[196:199], v[52:55]\n\t"
    "v_mfma_f32_16x16x32_bf16 v[52:55], a[124:127], v[212:215], v[52:55]\n\t"
    "v_mfma_f32_16x16x32_bf16 v[232:235], a[96:99], v[200:203], v[232:235]\n\t"
    "v_mfma_f32_16x16x32_bf16 v[232:235], a[112:115], v[216:219], v[232:235]\n\t"
    "v_mfma_f32_16x16x32_bf16 v[248:251], a[100:103], v[200:203], v[248:251]\n\t"
    "v_mfma_f32_16x16x32_bf16 v[248:251], a[116:119], v[216:219], v[248:251]\n\t"
    "s_waitcnt vmcnt(7)\n\t"
    "v_cmp_ne_u64 vcc, v[30:31], s[56:57]\n\t"
    "s_cmp_eq_u64 vcc, 0\n\t"
    "s_cbranch_scc1 131f\n\t"
    "s_mov_b32 s88, 0x40000\n\t"
    "130:\n\t"
    "s_add_u32 s89, s75, 262144\n\t"
    "v_mov_b64 v[30:31], s[56:57]\n\t"
    "s_mov_b64 exec, 0xff\n\t"
    "v_mov_b64 v[30:31], 0\n\t"
    "buffer_atomic_or_x2 v[30:31], v20, %[sw], s89 offen sc0\n\t"
    "s_mov_b64 exec, -1\n\t"
    "s_waitcnt vmcnt(0)\n\t"
    "v_cmp_ne_u64 vcc, v[30:31], s[56:57]\n\t"
    "s_cmp_eq_u64 vcc, 0\n\t"
    "s_cbranch_scc1 131f\n\t"
    "s_sub_u32 s88, s88, 1\n\t"
    "s_cmp_lg_u32 s88, 0\n\t"
    "s_cbranch_scc1 130b\n\t"
    "131:\n\t"
    "v_mfma_f32_16x16x32_bf16 v[120:123], a[104:107], v[200:203], v[120:123]\n\t"
    "global_load_dwordx4 v[132:135], v0, s[66:67] offset:-1024 sc1\n\t"
    "v_mfma_f32_16x16x32_bf16 v[120:123], a[120:123], v[216:219], v[120:123]\n\t"
    "v_mfma_f32_16x16x32_bf16 v[56:59], a[108:111], v[200:203], v[56:59]\n\t"
    "v_mfma_f32_16x16x32_bf16 v[56:59], a[124:127], v[216:219], v[56:59]\n\t"
    "global_load_dwordx4 v[148:151], v0, s[66:67] offset:2048 sc1\n\t"
    "v_mfma_f32_16x16x32_bf16 v[236:239], a[96:99], v[204:207], v[236:239]\n\t"
    "v_mfma_f32_16x16x32_bf16 v[236:239], a[112:115], v[220:223], v[236:239]\n\t"
    "v_mfma_f32_16x16x32_bf16 v[252:255], a[100:103], v[204:207], v[252:255]\n\t"
    "v_mfma_f32_16x16x32_bf16 v[252:255], a[116:119], v[220:223], v[252:255]\n\t"
    "global_load_dwordx4 v[164:167], v3, s[66:67] offset:-1024 sc1\n\t"
    "v_mfma_f32_16x16x32_bf16 v[124:127], a[104:107], v[204:207], v[124:127]\n\t"
    "v_mfma_f32_16x16x32_bf16 v[124:127], a[120:123], v[220:223], v[124:127]\n\t"
    "v_mfma_f32_16x16x32_bf16 v[60:63], a[108:111], v[204:207], v[60:63]\n\t"
    "v_mfma_f32_16x16x32_bf16 v[60:63], a[124:127], v[220:223], v[60:63]\n\t"
    "global_load_dwordx4 v[180:183], v3, s[66:67] offset:2048 sc1\n\t"
    "v_mfma_f32_16x16x32_bf16 v[224:227], a[96:99], v[192:195], v[224:227]\n\t"
    "v_mfma_f32_16x16x32_bf16 v[224:227], a[112:115], v[208:211], v[224:227]\n\t"
    "v_mfma_f32_16x16x32_bf16 v[240:243], a[100:103], v[192:195], v[240:243]\n\t"
    "v_mfma_f32_16x16x32_bf16 v[240:243], a[116:119], v[208:211], v[240:243]\n\t"
    "global_load_dwordx4 v[136:139], v0, s[96:97] offset:-1024 sc1\n\t"
    "v_mfma_f32_16x16x32_bf16 v[112:115], a[104:107], v[192:195], v[112:115]\n\t"
    "v_mfma_f32_16x16x32_bf16 v[112:115], a[120:123], v[208:211], v[112:115]\n\t"
    "v_mfma_f32_16x16x32_bf16 v[48:51], a[108:111], v[192:195], v[48:51]\n\t"
    "v_mfma_f32_16x16x32_bf16 v[48:51], a[124:127], v[208:211], v[48:51]\n\t"
    "s_waitcnt vmcnt(5)\n\t"
    "v_mfma_f32_16x16x32_bf16 v[228:231], a[128:131], a[228:231], v[228:231]\n\t"
    "global_load_dwordx4 v[152:155], v0, s[96:97] offset:2048 sc1\n\t"
    "v_mfma_f32_16x16x32_bf16 v[228:231], a[144:147], a[244:247], v[228:231]\n\t"
    "v_mfma_f32_16x16x32_bf16 v[244:247], a[132:135], a[228:231], v[244:247]\n\t"
    "v_mfma_f32_16x16x32_bf16 v[244:247], a[148:151], a[244:247], v[244:247]\n\t"
    "v_mfma_f32_16x16x32_bf16 v[116:119], a[136:139], a[228:231], v[116:119]\n\t"
    "global_load_dwordx4 v[168:171], v3, s[96:97] offset:-1024 sc1\n\t"
    "v_mfma_f32_16x16x32_bf16 v[116:119], a[152:155], a[244:247], v[116:119]\n\t"
    "v_mfma_f32_16x16x32_bf16 v[52:55], a[140:143], a[228:231], v[52:55]\n\t"
    "v_mfma_f32_16x16x32_bf16 v[52:55], a[156:159], a[244:247], v[52:55]\n\t"
    "v_mfma_f32_16x16x32_bf16 v[232:235], a[128:131], a[232:235], v[232:235]\n\t"
    "global_load_dwordx4 v[184:187], v3, s[96:97] offset:2048 sc1\n\t"
    "v_mfma_f32_16x16x32_bf16 v[232:235], a[144:147], a[248:251], v[232:235]\n\t"
    "v_mfma_f32_16x16x32_bf16 v[248:251], a[132:135], a[232:235], v[248:251]\n\t"
    "v_mfma_f32_16x16x32_bf16 v[248:251], a[148:151], a[248:251], v[248:251]\n\t"
    "v_mfma_f32_16x16x32_bf16 v[120:123], a[136:139], a[232:235], v[120:123]\n\t"
    "global_load_dwordx4 v[140:143], v0, s[98:99] offset:-1024 sc1\n\t"
    "v_mfma_f32_16x16x32_bf16 v[120:123], a[152:155], a[248:251], v[120:123]\n\t"
    "v_mfma_f32_16x16x32_bf16 v[56:59], a[140:143], a[232:235], v[56:59]\n\t"
    "v_mfma_f32_16x16x32_bf16 v[56:59], a[156:159], a[248:251], v[56:59]\n\t"
    "v_mfma_f32_16x16x32_bf16 v[236:239], a[128:131], a[236:239], v[236:239]\n\t"
    "global_load_dwordx4 v[156:159], v0, s[98:99] offset:2048 sc1\n\t"
    "v_mfma_f32_16x16x32_bf16 v[236:239], a[144:147], a[252:255], v[236:239]\n\t"
    "v_mfma_f32_16x16x32_bf16 v[252:255], a[132:135], a[236:239], v[252:255]\n\t"
    "v_mfma_f32_16x16x32_bf16 v[252:255], a[148:151], a[252:255], v[252:255]\n\t"
    "v_mfma_f32_16x16x32_bf16 v[124:127], a[136:139], a[236:239], v[124:127]\n\t"
    "v_mfma_f32_16x16x32_bf16 v[124:127], a[152:155], a[252:255], v[124:127]\n\t"
    "global_load_dwordx4 v[172:175], v3, s[98:99] offset:-1024 sc1\n\t"
    "v_mfma_f32_16x16x32_bf16 v[60:63], a[140:143], a[236:239], v[60:63]\n\t"
    "v_mfma_f32_16x16x32_bf16 v[60:63], a[156:159], a[252:255], v[60:63]\n\t"
    "v_mfma_f32_16x16x32_bf16 v[224:227], a[128:131], a[224:227], v[224:227]\n\t"
    "v_mfma_f32_16x16x32_bf16 v[224:227], a[144:147], a[240:243], v[224:227]\n\t"
    "global_load_dwordx4 v[188:191], v3, s[98:99] offset:2048 sc1\n\t"
    "v_mfma_f32_16x16x32_bf16 v[240:243], a[132:135], a[224:227], v[240:243]\n\t"
    "v_mfma_f32_16x16x32_bf16 v[240:243], a[148:151], a[240:243], v[240:243]\n\t"
    "v_mfma_f32_16x16x32_bf16 v[112:115], a[136:139], a[224:227], v[112:115]\n\t"
    "v_mfma_f32_16x16x32_bf16 v[112:115], a[152:155], a[240:243], v[112:115]\n\t"
    "global_load_dwordx4 v[128:131], v0, s[62:63] offset:-1024 sc1\n\t"
    "v_mfma_f32_16x16x32_bf16 v[48:51], a[140:143], a[224:227], v[48:51]\n\t"
    "v_mfma_f32_16x16x32_bf16 v[48:51], a[156:159], a[240:243], v[48:51]\n\t"
    "s_waitcnt lgkmcnt(0)\n\t"
    "s_waitcnt vmcnt(9)\n\t"
    "v_mfma_f32_16x16x32_bf16 v[228:231], a[160:163], v[132:135], v[228:231]\n\t"
    "v_mfma_f32_16x16x32_bf16 v[228:231], a[176:179], v[148:151], v[228:231]\n\t"
    "global_load_dwordx4 v[144:147], v0, s[62:63] offset:2048 sc1\n\t"
    "v_mfma_f32_16x16x32_bf16 v[228:231], a[192:195], v[164:167], v[228:231]\n\t"
    "v_mfma_f32_16x16x32_bf16 v[228:231], a[208:211], v[180:183], v[228:231]\n\t"
    "v_mfma_f32_16x16x32_bf16 v[244:247], a[164:167], v[132:135], v[244:247]\n\t"
    "v_mfma_f32_16x16x32_bf16 v[244:247], a[180:183], v[148:151], v[244:247]\n\t"
    "global_load_dwordx4 v[160:163], v3, s[62:63] offset:-1024 sc1\n\t"
    "v_mfma_f32_16x16x32_bf16 v[244:247], a[196:199], v[164:167], v[244:247]\n\t"
    "v_mfma_f32_16x16x32_bf16 v[244:247], a[212:215], v[180:183], v[244:247]\n\t"
    "v_mfma_f32_16x16x32_bf16 v[116:119], a[168:171], v[132:135], v[116:119]\n\t"
    "v_mfma_f32_16x16x32_bf16 v[116:119], a[184:187], v[148:151], v[116:119]\n\t"
    "v_mfma_f32_16x16x32_bf16 v[116:119], a[200:203], v[164:167], v[116:119]\n\t"
    "global_load_dwordx4 v[176:179], v3, s[62:63] offset:2048 sc1\n\t"
    "v_mfma_f32_16x16x32_bf16 v[116:119], a[216:219], v[180:183], v[116:119]\n\t"
    "ds_write_b128 v96, v[228:231] offset:0\n\t"
    "v_mfma_f32_16x16x32_bf16 v[52:55], a[172:175], v[132:135], v[52:55]\n\t"
    "v_mfma_f32_16x16x32_bf16 v[52:55], a[188:191], v[148:151], v[52:55]\n\t"
    "v_mfma_f32_16x16x32_bf16 v[52:55], a[204:207], v[164:167], v[52:55]\n\t"
    "buffer_load_dwordx4 v[80:83], v21, %[sw], 0 offen offset:0 sc1\n\t"
    "v_mfma_f32_16x16x32_bf16 v[52:55], a[220:223], v[180:183], v[52:55]\n\t"
    "ds_write_b128 v96, v[244:247] offset:1024\n\t"
    "s_waitcnt vmcnt(9)\n\t"
    "v_mfma_f32_16x16x32_bf16 v[232:235], a[160:163], v[136:139], v[232:235]\n\t"
    "v_mfma_f32_16x16x32_bf16 v[232:235], a[176:179], v[152:155], v[232:235]\n\t"
    "v_mfma_f32_16x16x32_bf16 v[232:235], a[192:195], v[168:171], v[232:235]\n\t"
    "v_mfma_f32_16x16x32_bf16 v[232:235], a[208:211], v[184:187], v[232:235]\n\t"
    "ds_write_b128 v96, v[116:119] offset:2048\n\t"
    "v_mfma_f32_16x16x32_bf16 v[248:251], a[164:167], v[136:139], v[248:251]\n\t"
    "v_mfma_f32_16x16x32_bf16 v[248:251], a[180:183], v[152:155], v[248:251]\n\t"
    "v_mfma_f32_16x16x32_bf16 v[248:251], a[196:199], v[168:171], v[248:251]\n\t"
    "v_mfma_f32_16x16x32_bf16 v[248:251], a[212:215], v[184:187], v[248:251]\n\t"
    "ds_write_b128 v96, v[52:55] offset:3072\n\t"
    "v_mfma_f32_16x16x32_bf16 v[120:123], a[168:171], v[136:139], v[120:123]\n\t"
    "v_mfma_f32_16x16x32_bf16 v[120:123], a[184:187], v[152:155], v[120:123]\n\t"
    "v_mfma_f32_16x16x32_bf16 v[120:123], a[200:203], v[168:171], v[120:123]\n\t"
    "v_mfma_f32_16x16x32_bf16 v[120:123], a[216:219], v[184:187], v[120:123]\n\t"
    "ds_write_b128 v97, v[232:235] offset:0\n\t"
    "v_mfma_f32_16x16x32_bf16 v[56:59], a[172:175], v[136:139], v[56:59]\n\t"
    "v_mfma_f32_16x16x32_bf16 v[56:59], a[188:191], v[152:155], v[56:59]\n\t"
    "v_mfma_f32_16x16x32_bf16 v[56:59], a[204:207], v[168:171], v[56:59]\n\t"
    "v_mfma_f32_16x16x32_bf16 v[56:59], a[220:223], v[184:187], v[56:59]\n\t"
    "buffer_load_dwordx4 v[84:87], v21, %[sw], 0 offen offset:16 sc1\n\t"
    "ds_write_b128 v97, v[248:251] offset:1024\n\t"
    "s_waitcnt vmcnt(6)\n\t"
    "v_mfma_f32_16x16x32_bf16 v[236:239], a[160:163], v[140:143], v[236:239]\n\t"
    "v_mfma_f32_16x16x32_bf16 v[236:239], a[176:179], v[156:159], v[236:239]\n\t"
    "v_mfma_f32_16x16x32_bf16 v[236:239], a[192:195], v[172:175], v[236:239]\n\t"
    "v_mfma_f32_16x16x32_bf16 v[236:239], a[208:211], v[188:191], v[236:239]\n\t"
    "ds_write_b128 v97, v[120:123] offset:2048\n\t"
    "v_mfma_f32_16x16x32_bf16 v[252:255], a[164:167], v[140:143], v[252:255]\n\t"
    "v_mfma_f32_16x16x32_bf16 v[252:255], a[180:183], v[156:159], v[252:255]\n\t"
    "v_mfma_f32_16x16x32_bf16 v[252:255], a[196:199], v[172:175], v[252:255]\n\t"
    "v_mfma_f32_16x16x32_bf16 v[252:255], a[212:215], v[188:191], v[252:255]\n\t"
    "ds_write_b128 v97, v[56:59] offset:3072\n\t"
    "v_mfma_f32_16x16x32_bf16 v[124:127], a[168:171], v[140:143], v[124:127]\n\t"
    "v_mfma_f32_16x16x32_bf16 v[124:127], a[184:187], v[156:159], v[124:127]\n\t"
    "v_mfma_f32_16x16x32_bf16 v[124:127], a[200:203], v[172:175], v[124:127]\n\t"
    "v_mfma_f32_16x16x32_bf16 v[124:127], a[216:219], v[188:191], v[124:127]\n\t"
    "ds_write_b128 v98, v[236:239] offset:0\n\t"
    "v_mfma_f32_16x16x32_bf16 v[60:63], a[172:175], v[140:143], v[60:63]\n\t"
    "v_mfma_f32_16x16x32_bf16 v[60:63], a[188:191], v[156:159], v[60:63]\n\t"
    "v_mfma_f32_16x16x32_bf16 v[60:63], a[204:207], v[172:175], v[60:63]\n\t"
    "v_mfma_f32_16x16x32_bf16 v[60:63], a[220:223], v[188:191], v[60:63]\n\t"
    "ds_write_b128 v98, v[252:255] offset:1024\n\t"
    "s_waitcnt vmcnt(2)\n\t"
    "v_mfma_f32_16x16x32_bf16 v[224:227], a[160:163], v[128:131], v[224:227]\n\t"
    "v_mfma_f32_16x16x32_bf16 v[224:227], a[176:179], v[144:147], v[224:227]\n\t"
    "v_mfma_f32_16x16x32_bf16 v[224:227], a[192:195], v[160:163], v[224:227]\n\t"
    "v_mfma_f32_16x16x32_bf16 v[224:227], a[208:211], v[176:179], v[224:227]\n\t"
    "ds_write_b128 v98, v[124:127] offset:2048\n\t"
    "v_mfma_f32_16x16x32_bf16 v[240:243], a[164:167], v[128:131], v[240:243]\n\t"
    "v_mfma_f32_16x16x32_bf16 v[240:243], a[180:183], v[144:147], v[240:243]\n\t"
    "v_mfma_f32_16x16x32_bf16 v[240:243], a[196:199], v[160:163], v[240:243]\n\t"
    "v_mfma_f32_16x16x32_bf16 v[240:243], a[212:215], v[176:179], v[240:243]\n\t"
    "ds_write_b128 v98, v[60:63] offset:3072\n\t"
    "v_mfma_f32_16x16x32_bf16 v[112:115], a[168:171], v[128:131], v[112:115]\n\t"
    "v_mfma_f32_16x16x32_bf16 v[112:115], a[184:187], v[144:147], v[112:115]\n\t"
    "v_mfma_f32_16x16x32_bf16 v[112:115], a[200:203], v[160:163], v[112:115]\n\t"
    "v_mfma_f32_16x16x32_bf16 v[112:115], a[216:219], v[176:179], v[112:115]\n\t"
    "v_mfma_f32_16x16x32_bf16 v[48:51], a[172:175], v[128:131], v[48:51]\n\t"
    "v_mfma_f32_16x16x32_bf16 v[48:51], a[188:191], v[144:147], v[48:51]\n\t"
    "v_mfma_f32_16x16x32_bf16 v[48:51], a[204:207], v[160:163], v[48:51]\n\t"
    "v_mfma_f32_16x16x32_bf16 v[48:51], a[220:223], v[176:179], v[48:51]\n\t"
    "s_waitcnt lgkmcnt(0)\n\t"
    "s_barrier\n\t"
    "ds_read_b128 v[128:131], v99 offset:0\n\t"
    "ds_read_b128 v[132:135], v99 offset:4096\n\t"
    "ds_read_b128 v[136:139], v99 offset:8192\n\t"
    "ds_read_b128 v[140:143], v99 offset:1024\n\t"
    "ds_read_b128 v[144:147], v99 offset:5120\n\t"
    "ds_read_b128 v[148:151], v99 offset:9216\n\t"
    "ds_read_b128 v[152:155], v99 offset:2048\n\t"
    "ds_read_b128 v[156:159], v99 offset:6144\n\t"
    "ds_read_b128 v[160:163], v99 offset:10240\n\t"
    "ds_read_b128 v[164:167], v99 offset:3072\n\t"
    "ds_read_b128 v[168:171], v99 offset:7168\n\t"
    "ds_read_b128 v[172:175], v99 offset:11264\n\t"
    "s_waitcnt vmcnt(0)\n\t"
    "s_nop 3\n\t"
    "v_lshlrev_b32 v176, 16, v80\n\t"
    "v_and_b32 v177, 0xffff0000, v80\n\t"
    "v_lshlrev_b32 v192, 16, v88\n\t"
    "v_and_b32 v193, 0xffff0000, v88\n\t"
    "v_lshlrev_b32 v178, 16, v81\n\t"
    "v_and_b32 v179, 0xffff0000, v81\n\t"
    "v_lshlrev_b32 v194, 16, v89\n\t"
    "v_and_b32 v195, 0xffff0000, v89\n\t"
    "v_lshlrev_b32 v180, 16, v82\n\t"
    "v_and_b32 v181, 0xffff0000, v82\n\t"
    "v_lshlrev_b32 v196, 16, v90\n\t"
    "v_and_b32 v197, 0xffff0000, v90\n\t"
    "v_lshlrev_b32 v182, 16, v83\n\t"
    "v_and_b32 v183, 0xffff0000, v83\n\t"
    "v_lshlrev_b32 v198, 16, v91\n\t"
    "v_and_b32 v199, 0xffff0000, v91\n\t"
    "v_lshlrev_b32 v184, 16, v84\n\t"
    "v_and_b32 v185, 0xffff0000, v84\n\t"
    "v_lshlrev_b32 v200, 16, v92\n\t"
    "v_and_b32 v201, 0xffff0000, v92\n\t"
    "v_lshlrev_b32 v186, 16, v85\n\t"
    "v_and_b32 v187, 0xffff0000, v85\n\t"
    "v_lshlrev_b32 v202, 16, v93\n\t"
    "v_and_b32 v203, 0xffff0000, v93\n\t"
    "v_lshlrev_b32 v188, 16, v86\n\t"
    "v_and_b32 v189, 0xffff0000, v86\n\t"
    "v_lshlrev_b32 v204, 16, v94\n\t"
    "v_and_b32 v205, 0xffff0000, v94\n\t"
    "v_lshlrev_b32 v190, 16, v87\n\t"
    "v_and_b32 v191, 0xffff0000, v87\n\t"
    "v_lshlrev_b32 v206, 16, v95\n\t"
    "v_and_b32 v207, 0xffff0000, v95\n\t"
    "s_waitcnt lgkmcnt(9)\n\t"
    "v_pk_add_f32 v[128:129], v[128:129], v[132:133]\n\t"
    "v_pk_add_f32 v[136:137], v[136:137], v[224:225]\n\t"
    "v_pk_add_f32 v[130:131], v[130:131], v[134:135]\n\t"
    "v_pk_add_f32 v[138:139], v[138:139], v[226:227]\n\t"
    "v_pk_add_f32 v[128:129], v[128:129], v[136:137]\n\t"
    "v_pk_add_f32 v[130:131], v[130:131], v[138:139]\n\t"
    "v_pk_fma_f32 v[128:129], v[128:129], v[192:193], v[176:177]\n\t"
    "v_pk_fma_f32 v[130:131], v[130:131], v[194:195], v[178:179]\n\t"
    "v_cvt_pk_bf16_f32 v208, v128, v129\n\t"
    "v_cvt_pk_bf16_f32 v209, v130, v131\n\t"
    "s_waitcnt lgkmcnt(6)\n\t"
    "v_pk_add_f32 v[140:141], v[140:141], v[144:145]\n\t"
    "v_pk_add_f32 v[148:149], v[148:149], v[240:241]\n\t"
    "v_pk_add_f32 v[142:143], v[142:143], v[146:147]\n\t"
    "v_pk_add_f32 v[150:151], v[150:151], v[242:243]\n\t"
    "v_pk_add_f32 v[140:141], v[140:141], v[148:149]\n\t"
    "v_pk_add_f32 v[142:143], v[142:143], v[150:151]\n\t"
    "v_pk_fma_f32 v[140:141], v[140:141], v[196:197], v[180:181]\n\t"
    "v_pk_fma_f32 v[142:143], v[142:143], v[198:199], v[182:183]\n\t"
    "v_cvt_pk_bf16_f32 v210, v140, v141\n\t"
    "v_cvt_pk_bf16_f32 v211, v142, v143\n\t"
    "s_nop 1\n\t"
    "v_permlane16_swap_b32 v208, v210\n\t"
    "v_permlane16_swap_b32 v209, v211\n\t"
    "buffer_store_dwordx4 v[208:211], v22, %[so], 0 offen offset:0\n\t"
    "s_waitcnt lgkmcnt(3)\n\t"
    "v_pk_add_f32 v[152:153], v[152:153], v[156:157]\n\t"
    "v_pk_add_f32 v[160:161], v[160:161], v[112:113]\n\t"
    "v_pk_add_f32 v[154:155], v[154:155], v[158:159]\n\t"
    "v_pk_add_f32 v[162:163], v[162:163], v[114:115]\n\t"
    "v_pk_add_f32 v[152:153], v[152:153], v[160:161]\n\t"
    "v_pk_add_f32 v[154:155], v[154:155], v[162:163]\n\t"
    "v_pk_fma_f32 v[152:153], v[152:153], v[200:201], v[184:185]\n\t"
    "v_pk_fma_f32 v[154:155], v[154:155], v[202:203], v[186:187]\n\t"
    "v_cvt_pk_bf16_f32 v212, v152, v153\n\t"
    "v_cvt_pk_bf16_f32 v213, v154, v155\n\t"
    "s_waitcnt lgkmcnt(0)\n\t"
    "v_pk_add_f32 v[164:165], v[164:165], v[168:169]\n\t"
    "v_pk_add_f32 v[172:173], v[172:173], v[48:49]\n\t"
    "v_pk_add_f32 v[166:167], v[166:167], v[170:171]\n\t"
    "v_pk_add_f32 v[174:175], v[174:175], v[50:51]\n\t"
    "v_pk_add_f32 v[164:165], v[164:165], v[172:173]\n\t"
    "v_pk_add_f32 v[166:167], v[166:167], v[174:175]\n\t"
    "v_pk_fma_f32 v[164:165], v[164:165], v[204:205], v[188:189]\n\t"
    "v_pk_fma_f32 v[166:167], v[166:167], v[206:207], v[190:191]\n\t"
    "v_cvt_pk_bf16_f32 v214, v164, v165\n\t"
    "v_cvt_pk_bf16_f32 v215, v166, v167\n\t"
    "s_nop 1\n\t"
    "v_permlane16_swap_b32 v212, v214\n\t"
    "v_permlane16_swap_b32 v213, v215\n\t"
    "buffer_store_dwordx4 v[212:215], v22, %[so], 0 offen offset:64\n\t"
      ".Lfb_end:"
      :
      : "{v[0:3]}"(u32x4{l16, pat, wdo, stw}),
        "{v[8:15]}"(u32x8{rps0, rps1, rpd0, rpd1, exr, ur, yst, cnto}),
        "{v[232:239]}"(u32x8{ca, cb, ex, wrm, xo, msa, mso, mfo}),
        "{v[240:247]}"(u32x8{ah, am, av, cntz, wmo, f1r, mscw, mpo}),
        "{v[248:251]}"(u32x4{kvo, kcd, zpa, f1w}),
        "{v[252:253]}"(u32x2{f1o, pad1}),
        "{s[60:67]}"(u32x8{wv, task, uso, um0, spare, wdso, b3so, stg}),
        "{s[40:47]}"(u32x8{sx_, si_, slin, sp_, sj_, sa2, sn2, spad}),
        "{s[48:55]}"(ptrs),
        "{v[196:203]}"(e0),
        "{v[204:219]}"(c2_),
        "{v[254:255]}"(pad3),
        "{v[224:227]}"(e5),
        [sw] "s"(rw), [sd] "s"(rd_), [sx] "s"(rx_), [so] "s"(mk(pout, 2u << 20)), [sb4] "s"(w < 2 ? rg_ : ru_),
        [swm] "s"(mk(pwm, task ? 3u << 20 : 0u)), [srp] "s"((lin & 1) ? ru_ : rg_), [swr] "s"(w == 0 ? rg_ : w == 1 ? ru_ : rd_)
      : "memory", "vcc", "scc", "s56", "s57", "s58", "s59", "s68", "s69", "s70", "s71", "s72", "s73", "s74", "s75", "s76", "s77", "s78", "s79", "s80", "s81", "s82", "s83", "s84", "s85", "s86", "s87", "s88", "s89", "s90", "s91", "s92", "s93", "s94", "s95", "s96", "s97", "s98", "s99", "a0", "a1", "a2", "a3", "a4", "a5", "a6", "a7", "a8", "a9", "a10", "a11", "a12", "a13", "a14", "a15", "a16", "a17", "a18", "a19", "a20", "a21", "a22", "a23", "a24", "a25", "a26", "a27", "a28", "a29", "a30", "a31", "a32", "a33", "a34", "a35", "a36", "a37", "a38", "a39", "a40", "a41", "a42", "a43", "a44", "a45", "a46", "a47", "a48", "a49", "a50", "a51", "a52", "a53", "a54", "a55", "a56", "a57", "a58", "a59", "a60", "a61", "a62", "a63", "a64", "a65", "a66", "a67", "a68", "a69", "a70", "a71", "a72", "a73", "a74", "a75", "a76", "a77", "a78", "a79", "a80", "a81", "a82", "a83", "a84", "a85", "a86", "a87", "a88", "a89", "a90", "a91", "a92", "a93", "a94", "a95", "a96", "a97", "a98", "a99", "a100", "a101", "a102", "a103", "a104", "a105", "a106", "a107", "a108", "a109", "a110", "a111", "a112", "a113", "a114", "a115", "a116", "a117", "a118", "a119", "a120", "a121", "a122", "a123", "a124", "a125", "a126", "a127", "a128", "a129", "a130", "a131", "a132", "a133", "a134", "a135", "a136", "a137", "a138", "a139", "a140", "a141", "a142", "a143", "a144", "a145", "a146", "a147", "a148", "a149", "a150", "a151", "a152", "a153", "a154", "a155", "a156", "a157", "a158", "a159", "a160", "a161", "a162", "a163", "a164", "a165", "a166", "a167", "a168", "a169", "a170", "a171", "a172", "a173", "a174", "a175", "a176", "a177", "a178", "a179", "a180", "a181", "a182", "a183", "a184", "a185", "a186", "a187", "a188", "a189", "a190", "a191", "a192", "a193", "a194", "a195", "a196", "a197", "a198", "a199", "a200", "a201", "a202", "a203", "a204", "a205", "a206", "a207", "a208", "a209", "a210", "a211", "a212", "a213", "a214", "a215", "a216", "a217", "a218", "a219", "a220", "a221", "a222", "a223", "a224", "a225", "a226", "a227", "a228", "a229", "a230", "a231", "a232", "a233", "a234", "a235", "a236", "a237", "a238", "a239", "a240", "a241", "a242", "a243", "a244", "a245", "a246", "a247", "a248", "a249", "a250", "a251", "a252", "a253", "a254", "a255", "v4", "v5", "v6", "v7", "v16", "v17", "v18", "v19", "v20", "v21", "v22", "v23", "v24", "v25", "v26", "v27", "v28", "v29", "v30", "v31", "v32", "v33", "v34", "v36", "v37", "v38", "v39", "v40", "v41", "v42", "v43", "v44", "v45", "v46", "v47", "v48", "v49", "v50", "v51", "v52", "v53", "v54", "v55", "v56", "v57", "v58", "v59", "v60", "v61", "v62", "v63", "v64", "v65", "v66", "v67", "v68", "v69", "v70", "v71", "v72", "v73", "v74", "v75", "v76", "v77", "v78", "v79", "v80", "v81", "v82", "v83", "v84", "v85", "v86", "v87", "v88", "v89", "v90", "v91", "v92", "v93", "v94", "v95", "v96", "v97", "v98", "v99", "v100", "v101", "v102", "v103", "v104", "v105", "v106", "v107", "v108", "v109", "v110", "v111", "v112", "v113", "v114", "v115", "v116", "v117", "v118", "v119", "v120", "v121", "v122", "v123", "v124", "v125", "v126", "v127", "v128", "v129", "v130", "v131", "v132", "v133", "v134", "v135", "v136", "v137", "v138", "v139", "v140", "v141", "v142", "v143", "v144", "v145", "v146", "v147", "v148", "v149", "v150", "v151", "v152", "v153", "v154", "v155", "v156", "v157", "v158", "v159", "v160", "v161", "v162", "v163", "v164", "v165", "v166", "v167", "v168", "v169", "v170", "v171", "v172", "v173", "v174", "v175", "v176", "v177", "v178", "v179", "v180", "v181", "v182", "v183", "v184", "v185", "v186", "v187", "v188", "v189", "v190", "v191", "v192", "v193", "v194", "v195", "v220", "v221", "v222", "v223", "v228", "v229", "v230", "v231");
  }
}

extern "C" void kernel_launch(void* const* d_in, const int* in_sizes, int n_in, void* d_out, int out_size, void* d_ws, size_t ws_size,
                              hipStream_t stream) {
  fused_block<<<256, 256, 0, stream>>>(d_in[0], d_in[1], d_in[2], d_in[3], d_in[4], d_in[5], d_in[7], d_in[8], d_in[9], d_in[10], d_in[11],
                                       d_out, d_ws);
}
```
